```python
import torch

def _softmax(x, axis=-1):
    return torch.softmax(x, dim=axis)
def _gelu(x):
    return torch.nn.functional.gelu(x, approximate="tanh")
def _silu(x):
    return torch.nn.functional.silu(x)
def _sigmoid(x):
    return torch.sigmoid(x)
def _layernorm(x, axis=-1, eps=1e-5):
    m = x.mean(dim=axis, keepdim=True)
    v = x.var(dim=axis, keepdim=True, unbiased=False)
    return (x - m) / torch.sqrt(v + eps)
def _scatter_add(tgt, idx, src, axis=0):
    return tgt.clone().index_add_(axis, idx, src)
def _segment_sum(src, seg, n):
    out = torch.zeros((n,) + src.shape[1:], dtype=src.dtype)
    return out.index_add_(0, seg, src)
def _pad(x, pw):
    flat = tuple(v for pair in reversed(pw) for v in pair)
    return torch.nn.functional.pad(x, flat)

kernel_name = 'gated_mix__mha_block__glu_gated_bilinear__prompt'

def setup_inputs():

    _w = (lambda a: (lambda t: t.to(torch.bfloat16) if t.is_floating_point() else t)(torch.as_tensor(a)))
    def _A_setup():
        B, S, D, H = 1, 2048, 512, 32
        return {
            "x": _w((lambda *s: torch.randn(*s, dtype=torch.bfloat16) * 0.05)(B, S, D)),
            "wqkv": _w((lambda *s: torch.randn(*s, dtype=torch.bfloat16) * 0.8)(D, 3 * D)),
            "wo": _w((lambda *s: torch.randn(*s, dtype=torch.bfloat16) * 0.05)(D, D)),
        }
    _A_in = _A_setup()

    def _B_setup():
        B, S, D, F = 1, 2048, 512, 1792
        return {
            "x": _w((lambda *s: torch.randn(*s, dtype=torch.bfloat16) * 0.05)(B, S, D)),
            "w_gate": _w((lambda *s: torch.randn(*s, dtype=torch.bfloat16) * 1.6)(D, F) * (D ** -0.5)),
            "w_up": _w((lambda *s: torch.randn(*s, dtype=torch.bfloat16) * 1.6)(D, F) * (D ** -0.5)),
            "w_down": _w((lambda *s: torch.randn(*s, dtype=torch.bfloat16) * 1.6)(F, D) * (F ** -0.5)),
        }
    _B_in = _B_setup()

    return {
        "A_x": _A_in["x"],
        "A_wqkv": _A_in["wqkv"],
        "A_wo": _A_in["wo"],
        "B_x": _B_in["x"],
        "B_w_gate": _B_in["w_gate"],
        "B_w_up": _B_in["w_up"],
        "B_w_down": _B_in["w_down"],
        "gate_w": _w((lambda *s: torch.randn(*s, dtype=torch.bfloat16) * 0.05)(512) * 16.0),
    }

def reference(A_x, A_wqkv, A_wo, B_x, B_w_gate, B_w_up, B_w_down, gate_w):
    def _A_ref(x, wqkv, wo):
        B, S, D = x.shape
        H = 32
        Dh = D // H
        qkv = (lambda x, *d: x.permute(*d))((x @ wqkv).reshape(B, S, 3, H, Dh), 2, 0, 3, 1, 4)
        q, k, v = qkv[0], qkv[1], qkv[2]
        scores = torch.einsum("bhqd,bhkd->bhqk", q, k) * (Dh ** -0.5)
        p = _softmax(scores, axis=-1)
        o = torch.einsum("bhqk,bhkd->bhqd", p, v)
        return (lambda x, *d: x.permute(*d))(o, 0, 2, 1, 3).reshape(B, S, D) @ wo

    def _B_ref(x, w_gate, w_up, w_down):
        g = torch.einsum("bsd,df->bsf", x, w_gate)
        u = torch.einsum("bsd,df->bsf", x, w_up)
        h = g * u
        return torch.einsum("bsf,fd->bsd", h, w_down)

    x = A_x
    g = _sigmoid((x * gate_w).sum(-1, keepdims=True))
    a = _A_ref(x, A_wqkv, A_wo)
    b = _B_ref(x, B_w_gate, B_w_up, B_w_down)
    return g * a + (1 - g) * b
```

```cpp
#include <hip/hip_runtime.h>
#include <hip/hip_bf16.h>
#include <stdint.h>
#include <type_traits>

typedef __bf16 bf16x8 __attribute__((ext_vector_type(8)));
typedef __bf16 bf16x16 __attribute__((ext_vector_type(16)));
typedef float f32x16 __attribute__((ext_vector_type(16)));
typedef float f32x8 __attribute__((ext_vector_type(8)));
typedef float f32x4 __attribute__((ext_vector_type(4)));
typedef unsigned u32x4 __attribute__((ext_vector_type(4)));
typedef unsigned u32x2 __attribute__((ext_vector_type(2)));
typedef short s16x4 __attribute__((ext_vector_type(4)));
#define LAS __attribute__((address_space(3)))
#define DI __device__ __forceinline__

constexpr int SEQ = 2048, DM = 512, NH = 32, FF = 1792;
constexpr size_t QF_OFF = 0;
constexpr size_t KV_OFF = 2u << 20;
constexpr size_t HF_OFF = 6u << 20;
constexpr size_t OF_OFF = 13u << 20;
constexpr size_t GT_OFF = 15u << 20;
constexpr size_t WS_USED = 16u << 20;
constexpr int LDS_BYTES = 81920;
constexpr int XS_OFF = 65536;
constexpr float QSCALE = 0.25f * 1.4426950408889634f;

struct Params {
    const char* x; const char* wqkv; const char* wo; const char* wg; const char* wu; const char* wd; const char* gw;
    char* out; char* ws;
};

template <int I> using IC = std::integral_constant<int, I>;
template <int B, int E, class F> DI void sfor(F&& f) { if constexpr (B < E) { f(IC<B>{}); sfor<B + 1, E>(f); } }

#define MFMA(a, b, c) __builtin_amdgcn_mfma_f32_32x32x16_bf16((a), (b), (c), 0, 0, 0)
#define SBAR() __builtin_amdgcn_sched_barrier(0)
#define PIN(x) asm volatile("" : "+v"(x))

DI void dma16(unsigned voff, const char* sbase, unsigned ldsdst) {
    unsigned keep;
    asm volatile("s_mov_b32 %0, m0\n\ts_mov_b32 m0, %3\n\ts_nop 0\n\tglobal_load_lds_dwordx4 %1, %2\n\ts_mov_b32 m0, %0"
                 : "=&s"(keep) : "v"(voff), "s"(sbase), "s"(ldsdst) : "memory");
}
template <int OFF> DI u32x4 gld16(unsigned voff, const char* sbase) {
    u32x4 r; asm volatile("global_load_dwordx4 %0, %1, %2 offset:%3" : "=v"(r) : "v"(voff), "s"(sbase), "n"(OFF) : "memory"); return r;
}
template <int OFF> DI u32x4 gld16_sc1(unsigned voff, const char* sbase) {
    u32x4 r; asm volatile("global_load_dwordx4 %0, %1, %2 offset:%3 sc1" : "=v"(r) : "v"(voff), "s"(sbase), "n"(OFF) : "memory"); return r;
}
template <int N> DI void waitv() { asm volatile("s_waitcnt vmcnt(%0)" ::"n"(N) : "memory"); }
template <int N> DI void waitv2(u32x4& a, u32x4& b) { asm volatile("s_waitcnt vmcnt(%2)" : "+v"(a), "+v"(b) : "n"(N) : "memory"); }
template <int N> DI void waitv4(u32x4& a, u32x4& b, u32x4& c, u32x4& d) { asm volatile("s_waitcnt vmcnt(%4)" : "+v"(a), "+v"(b), "+v"(c), "+v"(d) : "n"(N) : "memory"); }
template <int N> DI void wait_bar() { asm volatile("s_waitcnt vmcnt(%0) lgkmcnt(0)\n\ts_barrier" ::"n"(N) : "memory"); }
DI void lds_bar() { asm volatile("s_waitcnt lgkmcnt(0)\n\ts_barrier" ::: "memory"); }

DI unsigned cvtpk(float lo, float hi) {
    typedef __bf16 b2 __attribute__((ext_vector_type(2)));
    b2 r; r[0] = (__bf16)lo; r[1] = (__bf16)hi; return __builtin_bit_cast(unsigned, r);
}
DI u32x2 trread(unsigned addr) {
    typedef short v4i16_t __attribute__((ext_vector_type(4)));
    return __builtin_bit_cast(u32x2, __builtin_amdgcn_ds_read_tr16_b64_v4i16((LAS v4i16_t*)(uintptr_t)addr));
}
DI bf16x8 trfrag(unsigned a0, unsigned a1) {
    u32x2 lo = trread(a0), hi = trread(a1);
    u32x4 r = {lo[0], lo[1], hi[0], hi[1]};
    return __builtin_bit_cast(bf16x8, r);
}
DI u32x4 ldsr128(unsigned addr) { return *(const LAS u32x4*)(uintptr_t)addr; }
DI void ldsw128(unsigned addr, u32x4 v) { *(LAS u32x4*)(uintptr_t)addr = v; }
DI void st16(__amdgpu_buffer_rsrc_t rs, unsigned voff, unsigned soff, u32x4 v) { __builtin_amdgcn_raw_buffer_store_b128(v, rs, voff, soff, 0); }

struct WStream { const char* a; const char* b; unsigned va, vb; unsigned step; };

template <int NT, bool FIRST, bool TAIL>
DI void g1_pass(f32x16 (&acc)[4], u32x4 (&xf)[32], const unsigned (&ra_)[4], const WStream& cur, const WStream& nxt,
                unsigned ring, unsigned wdst, unsigned xw, unsigned xr) {
    bf16x8 wf[2][4];
    unsigned ra[4];
    sfor<0, NT>([&](auto T) { ra[T.value] = ra_[T.value]; PIN(ra[T.value]); });
    auto xshuffle = [&](auto CC, auto NW) {
        constexpr int c = CC.value;
        waitv4<NW.value>(xf[4 * c], xf[4 * c + 1], xf[4 * c + 2], xf[4 * c + 3]);
        ldsw128(xw, xf[4 * c]); ldsw128((xw ^ 64) + 1024, xf[4 * c + 1]); ldsw128(xw + 2048, xf[4 * c + 2]); ldsw128((xw ^ 64) + 3072, xf[4 * c + 3]);
        xf[4 * c] = ldsr128(xr); xf[4 * c + 1] = ldsr128(xr ^ 32); xf[4 * c + 2] = ldsr128(xr ^ 64); xf[4 * c + 3] = ldsr128(xr ^ 96);
    };
    auto reads = [&](auto NN) {
        constexpr int n = NN.value, off = ((n >> 1) & 7) * 8192 + (n & 1) * 2048;
        sfor<0, NT>([&](auto T) { wf[n & 1][T.value] = trfrag(ra[T.value] + off, ra[T.value] + off + 512); });
    };
    auto slot_top = [&](auto SS) {
        constexpr int s = SS.value;
        constexpr int ahead = TAIL ? ((15 - s) < 6 ? (15 - s) : 6) : 6;
        constexpr int xyoung = !FIRST ? 0 : (s > 6 ? 0 : 4 * (7 - (s >> 1)));
        wait_bar<2 * ahead + xyoung>();
        constexpr int t = s + 7;
        if constexpr (t < 16) {
            dma16(cur.va, cur.a + (size_t)t * cur.step, wdst + (t & 7) * 8192);
            dma16(cur.vb, cur.b + (size_t)t * cur.step, wdst + (t & 7) * 8192 + 4096);
        } else if constexpr (!TAIL) {
            dma16(nxt.va, nxt.a + (size_t)(t - 16) * nxt.step, wdst + (t & 7) * 8192);
            dma16(nxt.vb, nxt.b + (size_t)(t - 16) * nxt.step, wdst + (t & 7) * 8192 + 4096);
        }
    };
    if constexpr (FIRST) xshuffle(IC<0>{}, IC<42>{});
    slot_top(IC<0>{});
    reads(IC<0>{});
    sfor<0, 32>([&](auto NN) {
        constexpr int n = NN.value;
        if constexpr (n + 1 < 32) {
            if constexpr ((n & 1) == 1) slot_top(IC<(n + 1) / 2>{});
            reads(IC<n + 1>{});
        }
        if constexpr (FIRST && (n & 3) == 1 && (n >> 2) + 1 < 8) {
            constexpr int c = (n >> 2) + 1, s = (n + 1) / 2;
            constexpr int wdone = (c <= 3) ? 2 * c : 7;
            constexpr int young = 4 * (7 - c) + 2 * (s + 8 - wdone);
            xshuffle(IC<c>{}, IC<young>{});
        }
        SBAR();
        sfor<0, NT>([&](auto T) {
            if constexpr (n == 0) acc[T.value] = MFMA(wf[0][T.value], __builtin_bit_cast(bf16x8, xf[0]), f32x16{});
            else acc[T.value] = MFMA(wf[n & 1][T.value], __builtin_bit_cast(bf16x8, xf[n]), acc[T.value]);
        });
        SBAR();
    });
}

DI void g_map(int gi, int& mb, int& nb) { const int xc = gi & 7, wi = gi >> 3; mb = (xc >> 1) * 4 + (wi & 3); nb = (xc & 1) * 8 + (wi >> 2); }

DI void g1_phase(const Params& p, int gi, char* lds) {
    const int tid = threadIdx.x, l = tid & 63, w = __builtin_amdgcn_readfirstlane(tid >> 6);
    const int h = l >> 5, g = (l >> 4) & 1, q = (l >> 2) & 3, pp = l & 3;
    int mb, nb; g_map(gi, mb, nb);
    const unsigned ring = (unsigned)(uintptr_t)lds, wdst = ring + w * 1024;
    const int tt = mb * 4 + w;
    const unsigned swz = (((l >> 1) & 3) ^ (2 * ((l >> 4) & 1))) * 32 + (l & 1) * 16;
    WStream sq, sf0, sf1;
    { const int line0 = (3 * nb) >> 1;
      sq.a = p.wqkv + (size_t)w * 8 * 3072 + line0 * 128; sq.b = sq.a + 128; sq.va = (l >> 3) * 3072 + swz; sq.vb = (l >> 3) * 3072 + (swz ^ 32); sq.step = 32 * 3072; }
    const int grp0 = (nb >> 2) * 7 + (nb & 3) * 2, nffn = ((nb & 3) == 3) ? 1 : 2;
    sf0.a = p.wg + (size_t)w * 8 * 3584 + grp0 * 128; sf0.b = p.wu + (size_t)w * 8 * 3584 + grp0 * 128;
    sf0.va = (l >> 3) * 3584 + swz; sf0.vb = (l >> 3) * 3584 + (swz ^ 32); sf0.step = 32 * 3584;
    sf1 = sf0; sf1.a += 128; sf1.b += 128;
    u32x4 xf[32];
    const char* xb = p.x + (size_t)tt * 32 * 1024; const unsigned xv = (l >> 3) * 1024 + (l & 7) * 16;
    auto xload = [&](auto CC) { constexpr int c = CC.value;
        xf[4 * c] = gld16<c * 128>(xv, xb); xf[4 * c + 1] = gld16<c * 128>(xv, xb + 8192); xf[4 * c + 2] = gld16<c * 128>(xv, xb + 16384); xf[4 * c + 3] = gld16<c * 128>(xv, xb + 24576); };
    auto wload = [&](auto TT) { constexpr int t = TT.value;
        dma16(sq.va, sq.a + (size_t)t * sq.step, wdst + t * 8192); dma16(sq.vb, sq.b + (size_t)t * sq.step, wdst + t * 8192 + 4096); };
    xload(IC<0>{}); wload(IC<0>{}); wload(IC<1>{}); xload(IC<1>{}); wload(IC<2>{}); wload(IC<3>{}); xload(IC<2>{}); wload(IC<4>{}); wload(IC<5>{});
    xload(IC<3>{}); wload(IC<6>{}); xload(IC<4>{}); xload(IC<5>{}); xload(IC<6>{}); xload(IC<7>{});
    const unsigned xs = ring + XS_OFF + w * 4096;
    const unsigned xw = xs + (l >> 3) * 128 + (((l & 7) ^ (l >> 4)) * 16);
    const unsigned xr = xs + (l & 31) * 128 + ((h ^ ((l >> 1) & 7)) * 16);
    const unsigned rbase = ring + (8 * h + q) * 128 + pp * 8;
    const int qh2 = (q >> 1) * 2;
    const __amdgpu_buffer_rsrc_t rs = __builtin_amdgcn_make_buffer_rsrc(p.ws, 0, (int)WS_USED, 0x00020000);
    f32x16 acc[4];
    unsigned ra[4];
    for (int t = 0; t < 3; ++t) { const int tp = t + (nb & 1), line = tp >> 1; ra[t] = rbase + line * 4096 + (((2 * (tp & 1) + g) ^ qh2 ^ line) * 32); }
    ra[3] = 0;
    g1_pass<3, true, false>(acc, xf, ra, sq, sf0, ring, wdst, xw, xr);
    dma16(l * 16, p.gw, xs);
    sfor<0, 3>([&](auto T) {
        constexpr int t = T.value;
        const int tau = 3 * nb + t, kind = tau >> 4, hp = tau & 15;
        const float sc = kind == 0 ? QSCALE : 1.0f;
        const unsigned lo = kind == 2 ? (unsigned)(h * 512 + (((l & 31) ^ (4 * h)) * 16)) : (unsigned)(l * 16);
        const unsigned base = kind == 0 ? (unsigned)QF_OFF + (hp * 2 * 64 + tt) * 1024 : (unsigned)KV_OFF + (hp * 2 * 64 + tt) * 2048 + (kind - 1) * 1024;
        const unsigned hstride = kind == 0 ? 64 * 1024 : 64 * 2048;
        sfor<0, 2>([&](auto HH) { constexpr int o = HH.value * 8;
            u32x4 v = {cvtpk(acc[t][o] * sc, acc[t][o + 1] * sc), cvtpk(acc[t][o + 2] * sc, acc[t][o + 3] * sc), cvtpk(acc[t][o + 4] * sc, acc[t][o + 5] * sc), cvtpk(acc[t][o + 6] * sc, acc[t][o + 7] * sc)};
            st16(rs, lo, base + HH.value * hstride, v); });
    });
    for (int t = 0; t < 4; ++t) ra[t] = rbase + g * 4096 + ((t ^ qh2 ^ g) * 32);
    auto ffn_epi = [&](int grp) {
        sfor<0, 4>([&](auto T) { constexpr int t = T.value;
            u32x4 v = {cvtpk(acc[t][0] * acc[t][8], acc[t][1] * acc[t][9]), cvtpk(acc[t][2] * acc[t][10], acc[t][3] * acc[t][11]),
                       cvtpk(acc[t][4] * acc[t][12], acc[t][5] * acc[t][13]), cvtpk(acc[t][6] * acc[t][14], acc[t][7] * acc[t][15])};
            st16(rs, l * 16, (unsigned)HF_OFF + (tt * 112 + grp * 4 + t) * 1024, v); });
    };
    if (nffn == 2) {
        g1_pass<4, false, false>(acc, xf, ra, sf0, sf1, ring, wdst, xw, xr);
        ffn_epi(grp0);
        sf0 = sf1;
    }
    g1_pass<4, false, true>(acc, xf, ra, sf0, sf0, ring, wdst, xw, xr);
    ffn_epi(grp0 + nffn - 1);
    if (nb == 0) {
        f32x16 ga = {};
        sfor<0, 32>([&](auto S) { ga = MFMA(__builtin_bit_cast(bf16x8, ldsr128(xs + S.value * 32 + h * 16)), __builtin_bit_cast(bf16x8, xf[S.value]), ga); });
        const float gate = 1.0f / (1.0f + __expf(-ga[0]));
        if (h == 0) ((float*)(p.ws + GT_OFF))[tt * 32 + l] = gate;
    }
}

constexpr int G2_PD = 6;
constexpr int g2_young(int m) {
    int pos = 0, need = 0;
    for (int i = 0; i < 7; ++i) { pos += 2; if (i == m + 1) need = pos > need ? pos : need; }
    for (int i = 0; i < G2_PD; ++i) { pos += 2; if (i == m) need = pos > need ? pos : need; }
    for (int i = 0; i <= m; ++i) {
        if (i + 7 < 36) { pos += 2; if (i + 7 == m + 1) need = pos > need ? pos : need; }
        if (i + G2_PD < 28) { pos += 2; if (i + G2_PD == m) need = pos > need ? pos : need; }
    }
    return pos - need;
}

DI void g2_phase(const Params& p, int gi, char* lds) {
    const int tid = threadIdx.x, l = tid & 63, w = __builtin_amdgcn_readfirstlane(tid >> 6);
    const int h = l >> 5, g = (l >> 4) & 1, q = (l >> 2) & 3, pp = l & 3;
    int mb, nb; g_map(gi, mb, nb);
    const unsigned ring = (unsigned)(uintptr_t)lds, wdst = ring + w * 1024;
    const int tt0 = mb * 4 + (nb & 1) * 2, n0 = (nb >> 1) * 64;
    const unsigned wv = (l >> 3) * 1024 + (((l >> 1) & 3) ^ (2 * ((l >> 4) & 1))) * 32 + (l & 1) * 16;
    const char* wdb = p.wd + (size_t)w * 448 * 1024 + n0 * 2;
    const char* wob = p.wo + (size_t)w * 128 * 1024 + n0 * 2;
    auto wload = [&](auto MM) { constexpr int m = MM.value;
        const char* b = m < 28 ? wdb + m * 16384 : wob + (m - 28) * 16384;
        dma16(wv, b, wdst + (m & 7) * 8192); dma16(wv, b + 8192, wdst + (m & 7) * 8192 + 4096); };
    sfor<0, 7>(wload);
    const char* hb = p.ws + HF_OFF + ((size_t)tt0 * 112 + w * 28) * 1024;
    const unsigned lv = l * 16;
    u32x4 hf[8][2];
    auto hload = [&](auto MM) { constexpr int m = MM.value;
        hf[m & 7][0] = gld16<0>(lv, hb + m * 1024); hf[m & 7][1] = gld16<0>(lv, hb + 112 * 1024 + m * 1024); };
    sfor<0, G2_PD>(hload);
    unsigned ra[2];
    for (int t = 0; t < 2; ++t) ra[t] = wdst + (4 * h + q) * 128 + pp * 8 + (((2 * t + g) ^ ((q >> 1) * 2)) * 32);
    bf16x8 wf[2][2];
    auto reads = [&](auto MM, bf16x8 (&dst)[2]) { constexpr int off = (MM.value & 7) * 8192;
        dst[0] = trfrag(ra[0] + off, ra[0] + off + 4096); dst[1] = trfrag(ra[1] + off, ra[1] + off + 4096); };
    f32x16 accB[4], accA[4];
    waitv<12 + 2 * G2_PD>();
    reads(IC<0>{}, wf[0]);
    bf16x8 wfo[8][2];
    sfor<0, 28>([&](auto MM) {
        constexpr int m = MM.value;
        wload(IC<m + 7>{});
        if constexpr (m + G2_PD < 28) hload(IC<m + G2_PD>{});
        waitv2<g2_young(m)>(hf[m & 7][0], hf[m & 7][1]);
        if constexpr (m + 1 < 28) reads(IC<m + 1>{}, wf[(m + 1) & 1]); else reads(IC<28>{}, wfo[0]);
        SBAR();
        sfor<0, 4>([&](auto T) { constexpr int t = T.value;
            if constexpr (m == 0) accB[t] = MFMA(wf[0][t >> 1], __builtin_bit_cast(bf16x8, hf[0][t & 1]), f32x16{});
            else accB[t] = MFMA(wf[m & 1][t >> 1], __builtin_bit_cast(bf16x8, hf[m & 7][t & 1]), accB[t]); });
        SBAR();
    });
    wload(IC<35>{});
    const char* ob = p.ws + OF_OFF + ((size_t)tt0 * 32 + w * 8) * 1024;
    sfor<0, 8>([&](auto MM) { constexpr int m = MM.value;
        hf[m][0] = gld16<0>(lv, ob + m * 1024); hf[m][1] = gld16<0>(lv, ob + 32 * 1024 + m * 1024); });
    waitv<16>();
    sfor<1, 8>([&](auto MM) { reads(IC<28 + MM.value>{}, wfo[MM.value]); });
    sfor<0, 8>([&](auto MM) { constexpr int m = MM.value;
        waitv2<14 - 2 * m>(hf[m][0], hf[m][1]);
        sfor<0, 4>([&](auto T) { constexpr int t = T.value;
            if constexpr (m == 0) accA[t] = MFMA(wfo[0][t >> 1], __builtin_bit_cast(bf16x8, hf[0][t & 1]), f32x16{});
            else accA[t] = MFMA(wfo[m][t >> 1], __builtin_bit_cast(bf16x8, hf[m][t & 1]), accA[t]); });
    });
    const float* gt = (const float*)(p.ws + GT_OFF) + tt0 * 32 + (l & 31);
    const float g0 = gt[0], g1 = gt[32];
    sfor<0, 4>([&](auto T) { constexpr int t = T.value; const float gg = (t & 1) ? g1 : g0;
        for (int r = 0; r < 16; ++r) accA[t][r] = gg * accA[t][r] + (1.0f - gg) * accB[t][r]; });
    lds_bar();
    sfor<0, 4>([&](auto T) { constexpr int t = T.value;
        if (t != w) for (int r = 0; r < 4; ++r) {
            f32x4 v = {accA[t][4 * r], accA[t][4 * r + 1], accA[t][4 * r + 2], accA[t][4 * r + 3]};
            ldsw128(ring + ((t * 4 + w) * 4 + r) * 1024 + l * 16, __builtin_bit_cast(u32x4, v)); } });
    f32x16 own = w == 0 ? accA[0] : (w == 1 ? accA[1] : (w == 2 ? accA[2] : accA[3]));
    lds_bar();
    for (int o = 1; o < 4; ++o) { const int src = (w + o) & 3;
        for (int r = 0; r < 4; ++r) { f32x4 v = __builtin_bit_cast(f32x4, ldsr128(ring + ((w * 4 + src) * 4 + r) * 1024 + l * 16));
            own[4 * r] += v[0]; own[4 * r + 1] += v[1]; own[4 * r + 2] += v[2]; own[4 * r + 3] += v[3]; } }
    char* orow = p.out + ((size_t)(tt0 + (w & 1)) * 32 + (l & 31)) * 1024 + (n0 + (w >> 1) * 32) * 2 + h * 16;
    for (int i = 0; i < 4; i += 2) {
        unsigned a0 = cvtpk(own[4 * i], own[4 * i + 1]), a1 = cvtpk(own[4 * i + 2], own[4 * i + 3]);
        unsigned b0 = cvtpk(own[4 * i + 4], own[4 * i + 5]), b1 = cvtpk(own[4 * i + 6], own[4 * i + 7]);
        auto r0 = __builtin_amdgcn_permlane32_swap(a0, b0, false, false);
        auto r1 = __builtin_amdgcn_permlane32_swap(a1, b1, false, false);
        u32x4 v = {r0[0], r1[0], r0[1], r1[1]};
        *(u32x4*)(orow + i * 16) = v;
    }
}

constexpr int AT = 2192, ASLOT = 8 * AT;
DI f32x16 smf(u32x4 a, f32x8 b, f32x16 c, int idx) {
    return __builtin_amdgcn_smfmac_f32_32x32x32_bf16(__builtin_bit_cast(bf16x8, a), __builtin_bit_cast(bf16x16, b), c, idx, 0, 0);
}
DI f32x8 lo8(const f32x16& s) { return __builtin_shufflevector(s, s, 0, 1, 2, 3, 4, 5, 6, 7); }
DI f32x8 hi8(const f32x16& s) { return __builtin_shufflevector(s, s, 8, 9, 10, 11, 12, 13, 14, 15); }
template <int B, int E> DI void exps(f32x16& s) {
#pragma unroll
    for (int i = B; i < E; ++i) s[i] = __builtin_amdgcn_exp2f(s[i]);
    PIN(s);
}
DI float rowmax16(const f32x16& s) {
    float a = __builtin_fmaxf(s[0], s[1]);
#pragma unroll
    for (int i = 2; i < 16; ++i) a = __builtin_fmaxf(a, s[i]);
    return a;
}

DI void attn_phase(const Params& p, int ai, char* lds) {
    const int tid = threadIdx.x, l = tid & 63, w = __builtin_amdgcn_readfirstlane(tid >> 6);
    const int head = (ai & 7) * 4 + ((ai >> 3) & 3), qb = ai >> 5;
    const unsigned base = (unsigned)(uintptr_t)lds;
    const __amdgpu_buffer_rsrc_t rs = __builtin_amdgcn_make_buffer_rsrc(p.ws, 0, (int)WS_USED, 0x00020000);
    const unsigned lv = l * 16;
    const unsigned kvb = (unsigned)KV_OFF + head * 64 * 2048;
    auto ld = [&](unsigned soff) { return __builtin_amdgcn_raw_buffer_load_b128(rs, lv, soff, 0); };
    const int qt = qb * 8 + w * 2;
    const bf16x8 q0 = __builtin_bit_cast(bf16x8, ld((unsigned)QF_OFF + (head * 64 + qt) * 1024));
    const bf16x8 q1 = __builtin_bit_cast(bf16x8, ld((unsigned)QF_OFF + (head * 64 + qt + 1) * 1024));
    if (tid < 32) { u32x2 one = {0x3f803f80u, 0x3f803f80u}; *(LAS u32x2*)(uintptr_t)(base + (tid >> 1) * AT + 2048 + (tid & 1) * 128) = one; }
    const int ha = l >> 5, g = (l >> 4) & 1, q = (l >> 2) & 3, pp = l & 3, hs = pp & 1;
    const unsigned wa = base + 2 * w * AT + lv;
    int idx = 0xDDDD; PIN(idx);
    int nmax = 1;
    for (int attempt = 0; attempt < 2; ++attempt) {
        u32x4 st[4];
        for (int i = 0; i < 4; ++i) st[i] = ld(kvb + w * 4096 + i * 1024);
        float m0 = -INFINITY, m1 = -INFINITY;
        for (int t = 0; t < nmax; ++t) {
            const bf16x8 kf = __builtin_bit_cast(bf16x8, ld(kvb + t * 2048));
            m0 = __builtin_fmaxf(m0, rowmax16(MFMA(kf, q0, f32x16{})));
            m1 = __builtin_fmaxf(m1, rowmax16(MFMA(kf, q1, f32x16{})));
        }
        m0 = __builtin_fmaxf(m0, __shfl_xor(m0, 32)); m1 = __builtin_fmaxf(m1, __shfl_xor(m1, 32));
        f32x16 c0, c1;
        for (int i = 0; i < 16; ++i) { c0[i] = -m0; c1[i] = -m1; }
        PIN(c0); PIN(c1);
        ldsw128(wa, st[0]); ldsw128(wa + 1024, st[1]); ldsw128(wa + AT, st[2]); ldsw128(wa + AT + 1024, st[3]);
        for (int i = 0; i < 4; ++i) st[i] = ld(kvb + 16384 + w * 4096 + i * 1024);
        unsigned ka = base + lv, va[4];
        for (int i = 0; i < 4; ++i) { const int s = i >> 1, j = i & 1;
            va[i] = base + (g ? (unsigned)(2048 + (1 - ha) * 128) : (unsigned)(1024 + hs * 512 + (((16 * s + 8 * ha + 4 * j + q) ^ (4 * hs)) * 16) + 8 * (pp >> 1))); }
        lds_bar();
        f32x16 o0 = {}, o1 = {}, sa, sb;
        f32x8 ph = {};
        u32x4 kc, kn, v0, v1, v0n, v1n, v1p = {};
        auto rdK = [&](int off) { return ldsr128(ka + off); };
        auto rdV = [&](int s, int off) { u32x2 a = trread(va[2 * s] + off), b = trread(va[2 * s + 1] + off); u32x4 r = {a[0], a[1], b[0], b[1]}; return r; };
        kc = rdK(0); v0 = rdV(0, 0); v1 = rdV(1, 0);
        sa = MFMA(__builtin_bit_cast(bf16x8, kc), q0, c0);
        int delta = ASLOT;
        for (int c = 0; c < 8; ++c) {
            sfor<0, 8>([&](auto T) {
                constexpr int t = T.value;
                if constexpr (t == 7) {
                    lds_bar();
                    ka += delta; va[0] += delta; va[1] += delta; va[2] += delta; va[3] += delta; delta = -delta;
                }
                constexpr int off = t == 7 ? 0 : (t + 1) * AT;
                SBAR();
                kn = rdK(off);
                o1 = smf(v1p, ph, o1, idx); exps<0, 5>(sa); SBAR();
                v0n = rdV(0, off);
                sb = MFMA(__builtin_bit_cast(bf16x8, kc), q1, c1); exps<5, 11>(sa); SBAR();
                v1n = rdV(1, off);
                o0 = smf(v0, lo8(sa), o0, idx); exps<11, 16>(sa); SBAR();
                if constexpr (t == 2) {
                    const unsigned wd = wa + ((c & 1) ? 0 : ASLOT);
                    ldsw128(wd, st[0]); ldsw128(wd + 1024, st[1]); ldsw128(wd + AT, st[2]); ldsw128(wd + AT + 1024, st[3]);
                    const int cn = c + 2 < 8 ? c + 2 : 7;
                    for (int i = 0; i < 4; ++i) st[i] = ld(kvb + cn * 16384 + w * 4096 + i * 1024);
                    SBAR();
                }
                o0 = smf(v1, hi8(sa), o0, idx); exps<0, 5>(sb); SBAR();
                sa = MFMA(__builtin_bit_cast(bf16x8, kn), q0, c0); exps<5, 11>(sb); SBAR();
                o1 = smf(v0, lo8(sb), o1, idx); exps<11, 16>(sb); SBAR();
                ph = hi8(sb); v1p = v1; kc = kn; v0 = v0n; v1 = v1n;
            });
        }
        o1 = smf(v1p, ph, o1, idx);
        float chk = __builtin_fmaxf(o0[8], o1[8]);
        bool bad = !(o0[8] < 1.0e18f) || !(o1[8] < 1.0e18f);
        for (int i = 0; i < 8; ++i) bad = bad || !(__builtin_fabsf(o0[i]) < 3.0e38f) || !(__builtin_fabsf(o1[i]) < 3.0e38f);
        (void)chk;
        const int anybad = attempt == 0 ? __syncthreads_or(bad ? 1 : 0) : 0;
        if (!anybad) {
            const float r0 = 1.0f / o0[8], r1 = 1.0f / o1[8];
            u32x4 a = {cvtpk(o0[0] * r0, o0[1] * r0), cvtpk(o0[2] * r0, o0[3] * r0), cvtpk(o0[4] * r0, o0[5] * r0), cvtpk(o0[6] * r0, o0[7] * r0)};
            u32x4 b = {cvtpk(o1[0] * r1, o1[1] * r1), cvtpk(o1[2] * r1, o1[3] * r1), cvtpk(o1[4] * r1, o1[5] * r1), cvtpk(o1[6] * r1, o1[7] * r1)};
            st16(rs, lv, (unsigned)OF_OFF + (qt * 32 + head) * 1024, a);
            st16(rs, lv, (unsigned)OF_OFF + ((qt + 1) * 32 + head) * 1024, b);
            break;
        }
        nmax = 64;
    }
}

__global__ __launch_bounds__(256, 2) void k_g1(Params p) { extern __shared__ __attribute__((aligned(16))) char lds[]; g1_phase(p, blockIdx.x, lds); }
__global__ __launch_bounds__(256, 2) void k_attn(Params p) { extern __shared__ __attribute__((aligned(16))) char lds[]; attn_phase(p, blockIdx.x, lds); }
__global__ __launch_bounds__(256, 2) void k_g2(Params p) { extern __shared__ __attribute__((aligned(16))) char lds[]; g2_phase(p, blockIdx.x, lds); }

extern "C" void kernel_launch(void* const* d_in, const int* in_sizes, int n_in, void* d_out, int out_size, void* d_ws, size_t ws_size,
                              hipStream_t stream) {
    Params p;
    p.x = (const char*)d_in[0]; p.wqkv = (const char*)d_in[1]; p.wo = (const char*)d_in[2];
    p.wg = (const char*)d_in[4]; p.wu = (const char*)d_in[5]; p.wd = (const char*)d_in[6]; p.gw = (const char*)d_in[7];
    p.out = (char*)d_out; p.ws = (char*)d_ws;
    (void)hipFuncSetAttribute((const void*)k_g1, hipFuncAttributeMaxDynamicSharedMemorySize, LDS_BYTES);
    (void)hipFuncSetAttribute((const void*)k_attn, hipFuncAttributeMaxDynamicSharedMemorySize, LDS_BYTES);
    (void)hipFuncSetAttribute((const void*)k_g2, hipFuncAttributeMaxDynamicSharedMemorySize, LDS_BYTES);
    k_g1<<<256, 256, LDS_BYTES, stream>>>(p);
    k_attn<<<256, 256, LDS_BYTES, stream>>>(p);
    k_g2<<<256, 256, LDS_BYTES, stream>>>(p);
}
```

```cpp
#include <hip/hip_runtime.h>
#include <hip/hip_bf16.h>
#include <stdint.h>
#include <type_traits>

typedef __bf16 bf16x8 __attribute__((ext_vector_type(8)));
typedef __bf16 bf16x16 __attribute__((ext_vector_type(16)));
typedef float f32x16 __attribute__((ext_vector_type(16)));
typedef float f32x8 __attribute__((ext_vector_type(8)));
typedef float f32x4 __attribute__((ext_vector_type(4)));
typedef unsigned u32x4 __attribute__((ext_vector_type(4)));
typedef unsigned u32x2 __attribute__((ext_vector_type(2)));
typedef unsigned long long u64;
typedef __amdgpu_buffer_rsrc_t rsrc_t;
#define LAS __attribute__((address_space(3)))
#define GAS __attribute__((address_space(1)))
#define DI __device__ __forceinline__

constexpr unsigned QF_OFF = 0;
constexpr unsigned KVT = 2176;
constexpr unsigned KV_OFF = 2u << 20;
constexpr unsigned HF_OFF = 7u << 20;
constexpr unsigned OF_OFF = 14u << 20;
constexpr unsigned FL_OFF = 16u << 20;
constexpr unsigned F1_OFF = FL_OFF + 2048;
constexpr unsigned F0_OFF = FL_OFF + 4096;
constexpr unsigned F2_OFF = FL_OFF + 6144;
constexpr unsigned F3_OFF = FL_OFF + 8192;
constexpr unsigned WS_USED = 17u << 20;
constexpr int LDS_BYTES = 81920;
constexpr int XS_OFF = 65536;
constexpr float QSCALE = 0.25f * 1.4426950408889634f;
constexpr unsigned SPIN_CAP = 1u << 18;
#define APRIO 2
#define GPRIO 0

struct Params {
    const char* x; const char* wqkv; const char* wo; const char* wg; const char* wu; const char* wd; const char* gw;
    char* out; char* ws;
};

template <int I> using IC = std::integral_constant<int, I>;
template <int B, int E, class F> DI void sfor(F&& f) { if constexpr (B < E) { f(IC<B>{}); sfor<B + 1, E>(f); } }

#define FSTAMP(i)
#define MFMA(a, b, c) __builtin_amdgcn_mfma_f32_32x32x16_bf16((a), (b), (c), 0, 0, 0)
#define SBAR() __builtin_amdgcn_sched_barrier(0)
#define PIN(x) asm volatile("" : "+v"(x))
#define RLX_AGENT __ATOMIC_RELAXED, __HIP_MEMORY_SCOPE_AGENT
#define BF8(x) __builtin_bit_cast(bf16x8, (x))

DI u64 rfl64(u64 v) { return ((u64)(unsigned)__builtin_amdgcn_readfirstlane((int)(v >> 32)) << 32) | (unsigned)__builtin_amdgcn_readfirstlane((int)v); }
DI rsrc_t mkrs(const char* p, unsigned bytes) { return __builtin_amdgcn_make_buffer_rsrc((void*)rfl64((u64)p), 0, (int)bytes, 0x00020000); }

struct WStream { rsrc_t sa, sb; unsigned va, vb, so, step; };
template <int DST> DI void wdma(WStream& s, unsigned wdst) {
    asm volatile("s_add_u32 m0, %[wd], %[imm]\n\ts_add_u32 %[so], %[so], %[st]\n\t"
                 "buffer_load_dwordx4 %[va], %[sa], %[so] offen lds\n\tbuffer_load_dwordx4 %[vb], %[sb], %[so] offen offset:1024 lds"
                 : [so] "+s"(s.so) : [wd] "s"(wdst), [imm] "n"(DST), [st] "s"(s.step), [va] "v"(s.va), [vb] "v"(s.vb), [sa] "s"(s.sa), [sb] "s"(s.sb)
                 : "scc", "memory");
}
template <int OFF> DI u32x4 gld16(unsigned voff, const char* sbase) {
    u32x4 r; asm volatile("global_load_dwordx4 %0, %1, %2 offset:%3" : "=v"(r) : "v"(voff), "s"(sbase), "n"(OFF) : "memory"); return r;
}
DI void hld2(u32x4& a, u32x4& b, unsigned voff, rsrc_t sa, rsrc_t sb, unsigned& so) {
    asm volatile("buffer_load_dwordx4 %0, %3, %4, %2 offen sc1\n\tbuffer_load_dwordx4 %1, %3, %5, %2 offen sc1"
                 : "=&v"(a), "=&v"(b) : "s"(so), "v"(voff), "s"(sa), "s"(sb) : "memory");
    so += 1024;
}
template <int N> DI void waitv() { asm volatile("s_waitcnt vmcnt(%0)" ::"n"(N) : "memory"); }
template <int N> DI void waitv2(u32x4& a, u32x4& b) { asm volatile("s_waitcnt vmcnt(%2)" : "+v"(a), "+v"(b) : "n"(N) : "memory"); }
template <int N> DI void waitv4(u32x4& a, u32x4& b, u32x4& c, u32x4& d) { asm volatile("s_waitcnt vmcnt(%4)" : "+v"(a), "+v"(b), "+v"(c), "+v"(d) : "n"(N) : "memory"); }
template <int N> DI void waitv_bar() { asm volatile("s_waitcnt vmcnt(%0)\n\ts_barrier" ::"n"(N) : "memory"); }
DI void lds_bar() { asm volatile("s_waitcnt lgkmcnt(0)\n\ts_barrier" ::: "memory"); }
DI void drain_bar() { asm volatile("s_waitcnt vmcnt(0) lgkmcnt(0)\n\ts_barrier" ::: "memory"); }

DI unsigned cvtpk(float lo, float hi) {
    typedef __bf16 b2 __attribute__((ext_vector_type(2)));
    b2 r; r[0] = (__bf16)lo; r[1] = (__bf16)hi; return __builtin_bit_cast(unsigned, r);
}
DI u32x2 trread(unsigned addr) {
    typedef short v4i16_t __attribute__((ext_vector_type(4)));
    return __builtin_bit_cast(u32x2, __builtin_amdgcn_ds_read_tr16_b64_v4i16((LAS v4i16_t*)(uintptr_t)addr));
}
DI u32x4 trfrag(unsigned a0, unsigned a1) { u32x2 lo = trread(a0), hi = trread(a1); u32x4 r = {lo[0], lo[1], hi[0], hi[1]}; return r; }
DI u32x4 ldsr128(unsigned addr) { return *(const LAS u32x4*)(uintptr_t)addr; }
DI void ldsw128(unsigned addr, u32x4 v) { *(LAS u32x4*)(uintptr_t)addr = v; }
DI void st16_wt(rsrc_t rs, unsigned voff, unsigned soff, u32x4 v) { __builtin_amdgcn_raw_buffer_store_b128(v, rs, voff, soff, 16); }
DI u32x4 ld16_dev(rsrc_t rs, unsigned voff, unsigned soff) { return __builtin_amdgcn_raw_buffer_load_b128(rs, voff, soff, 16); }

DI u64 mix64(u64 z) { z += 0x9E3779B97F4A7C15ull; z = (z ^ (z >> 30)) * 0xBF58476D1CE4E5B9ull; z = (z ^ (z >> 27)) * 0x94D049BB133111EBull; return z ^ (z >> 31); }
DI u64 ld_flag(const char* a) { return __hip_atomic_load((const GAS u64*)a, RLX_AGENT); }
DI void st_flag(char* a, u64 v) { __hip_atomic_store((GAS u64*)a, v, RLX_AGENT); }
template <int SLEEP> DI void wait_flags(const char* a, u64 tag) {
#pragma nounroll
    for (unsigned spins = 0; spins < SPIN_CAP; ++spins) {
        if (__all(ld_flag(a) == tag)) break;
        __builtin_amdgcn_s_sleep(SLEEP);
    }
    asm volatile("" ::: "memory");
}

constexpr int f_endx(int c) { return c < 3 ? 8 * c + 4 : 32 + 8 * (c - 3); }
constexpr int f_endw(int s) { return s < 6 ? 8 * (s >> 1) + 4 + 2 * ((s & 1) + 1) : 26 + 4 * (s - 6); }
constexpr int f_issued(int s) { return 24 + 4 * (s < 10 ? s : 10); }

template <bool FIRST, int NT>
DI void g1_pass(f32x16 (&acc)[4], u32x4 (&xf)[32], const unsigned (&ra_)[4], WStream& cur, WStream& nxt, unsigned wdst, unsigned xw, unsigned xr,
                unsigned xv, const char* xb) {
    u32x4 wf[2][4];
    unsigned ra[4];
    sfor<0, NT>([&](auto T) { ra[T.value] = ra_[T.value]; PIN(ra[T.value]); });
    auto xsh_w = [&](auto CC, auto NW) {
        constexpr int c = CC.value;
        waitv4<NW.value>(xf[4 * c], xf[4 * c + 1], xf[4 * c + 2], xf[4 * c + 3]);
        ldsw128(xw, xf[4 * c]); ldsw128((xw ^ 64) + 1024, xf[4 * c + 1]); ldsw128(xw + 2048, xf[4 * c + 2]); ldsw128((xw ^ 64) + 3072, xf[4 * c + 3]);
    };
    auto xsh_r = [&](auto CC) {
        constexpr int c = CC.value;
        xf[4 * c] = ldsr128(xr); xf[4 * c + 1] = ldsr128(xr ^ 32); xf[4 * c + 2] = ldsr128(xr ^ 64); xf[4 * c + 3] = ldsr128(xr ^ 96);
    };
    auto rd = [&](auto NN, auto T) {
        constexpr int n = NN.value, off = ((n >> 1) & 7) * 8192 + (n & 1) * 4096;
        wf[n & 1][T.value] = trfrag(ra[T.value] + off, ra[T.value] + off + 512);
    };
    auto bar = [&](auto SS) {
        constexpr int s = SS.value;
        waitv_bar<FIRST ? f_issued(s) - f_endw(s) : 10>();
    };
    auto dma = [&](auto SS) {
        constexpr int s = SS.value, t = s + 6;
        if constexpr (t < 16) wdma<(t & 7) * 8192>(cur, wdst);
        else if constexpr (!FIRST) wdma<(t & 7) * 8192>(nxt, wdst);
    };
    if constexpr (FIRST) { xsh_w(IC<0>{}, IC<f_issued(0) - f_endx(0)>{}); xsh_r(IC<0>{}); }
    bar(IC<0>{});
    sfor<0, NT>([&](auto T) { rd(IC<0>{}, T); });
    dma(IC<0>{});
    SBAR();
    sfor<0, 32>([&](auto NN) {
        constexpr int n = NN.value;
        constexpr bool xs_here = FIRST && (n & 3) == 2 && (n + 2) / 4 < 8;
        constexpr int xc = ((n + 2) / 4) & 7;
        constexpr bool xl_here = FIRST && (n & 1) == 0 && n < 20;
        constexpr int lc = (3 + n / 4) & 7, lp = n & 2;
        sfor<0, NT>([&](auto T) {
            constexpr int t = T.value;
            if constexpr (n == 0) acc[t] = MFMA(BF8(wf[0][t]), BF8(xf[0]), f32x16{});
            else acc[t] = MFMA(BF8(wf[n & 1][t]), BF8(xf[n]), acc[t]);
            SBAR();
            if constexpr (n + 1 < 32) {
                if constexpr ((n & 1) == 1 && t == 0) bar(IC<(n + 1) / 2>{});
                rd(IC<n + 1>{}, T);
                if constexpr ((n & 1) == 1 && t == 1) dma(IC<(n + 1) / 2>{});
            }
            if constexpr (xs_here && t == 0) xsh_w(IC<xc>{}, IC<f_issued(n / 2) + (n / 2 < 10 ? 2 : 0) - f_endx(xc)>{});
            if constexpr (xs_here && t == 2) xsh_r(IC<xc>{});
            if constexpr (xl_here && (t & 1) == 1) xf[4 * lc + lp + (t >> 1)] = gld16<lc * 128>(xv, xb + (lp + (t >> 1)) * 8192);
            SBAR();
        });
    });
}

DI void g_role(const Params& p, int gi, char* lds) {
    const int tid = threadIdx.x, l = tid & 63, w = __builtin_amdgcn_readfirstlane(tid >> 6);
    const int h = l >> 5, g = (l >> 4) & 1, q = (l >> 2) & 3, pp = l & 3;
    const int mb = ((gi & 7) >> 1) * 4 + ((gi >> 3) & 3), nb = (gi >> 5) * 2 + (gi & 1);
    const unsigned ring = (unsigned)(uintptr_t)lds, wdst = ring + w * 2048;
    const int tt = mb * 4 + w;
    FSTAMP(0);
    const bool hasq = nb < 12;
    const unsigned swz = (((l >> 1) & 3) ^ (2 * ((l >> 4) & 1))) * 32 + (l & 1) * 16;
    auto ffn_stream = [&](int gq) { WStream s;
        s.sa = mkrs(p.wg + gq * 128, 0x7fffffffu); s.sb = mkrs(p.wu + gq * 128 - 1024, 0x7fffffffu);
        s.va = (8 * w + (l >> 3)) * 3584 + swz; s.vb = s.va ^ 32; s.step = 32 * 3584; s.so = 0u - s.step; return s; };
    WStream cur, nxt;
    if (hasq) { cur.sa = mkrs(p.wqkv + nb * 256, 0x7fffffffu); cur.sb = mkrs(p.wqkv + nb * 256 + 128 - 1024, 0x7fffffffu);
        cur.va = (8 * w + (l >> 3)) * 3072 + swz; cur.vb = cur.va ^ 32; cur.step = 32 * 3072; cur.so = 0u - cur.step; }
    else cur = ffn_stream(4 + nb);
    u32x4 xf[32];
    const char* xb = p.x + (size_t)tt * 32 * 1024; const unsigned xv = (l >> 3) * 1024 + (l & 7) * 16;
    u64 epv;
    asm volatile("global_load_dwordx2 %0, %1, %2 sc1" : "=v"(epv) : "v"(0u), "s"(p.ws + FL_OFF) : "memory");
    sfor<0, 3>([&](auto CC) { constexpr int c = CC.value;
        xf[4 * c] = gld16<c * 128>(xv, xb); xf[4 * c + 1] = gld16<c * 128>(xv, xb + 8192); xf[4 * c + 2] = gld16<c * 128>(xv, xb + 16384); xf[4 * c + 3] = gld16<c * 128>(xv, xb + 24576);
        wdma<c * 16384>(cur, wdst); wdma<c * 16384 + 8192>(cur, wdst); });
    const unsigned xs = ring + XS_OFF + w * 4096;
    const unsigned xw = xs + (l >> 3) * 128 + (((l & 7) ^ (l >> 4)) * 16);
    const unsigned xr = xs + (l & 31) * 128 + ((h ^ ((l >> 1) & 7)) * 16);
    const unsigned rbase = ring + h * 2048 + q * 128 + pp * 8;
    const int qh2 = (q >> 1) * 2;
    const rsrc_t rs = mkrs(p.ws, WS_USED);
    const unsigned lv = l * 16;
    f32x16 acc[4];
    unsigned ra[4], raf[4];
    for (int t = 0; t < 4; ++t) {
        raf[t] = rbase + g * 1024 + ((t ^ qh2 ^ g) * 32);
        ra[t] = hasq ? rbase + (t >> 1) * 1024 + (((2 * (t & 1) + g) ^ qh2 ^ (t >> 1)) * 32) : raf[t];
    }
    FSTAMP(1);
    g1_pass<true, 4>(acc, xf, ra, cur, cur, wdst, xw, xr, xv, xb);
    FSTAMP(2);
    PIN(epv);
    const u64 tag = mix64(rfl64(epv));
    auto ffn_epi = [&](auto NT, int ks0) {
        sfor<0, NT.value>([&](auto T) { constexpr int t = T.value;
            u32x4 v = {cvtpk(acc[t][0] * acc[t][8], acc[t][1] * acc[t][9]), cvtpk(acc[t][2] * acc[t][10], acc[t][3] * acc[t][11]),
                       cvtpk(acc[t][4] * acc[t][12], acc[t][5] * acc[t][13]), cvtpk(acc[t][6] * acc[t][14], acc[t][7] * acc[t][15])};
            st16_wt(rs, lv, HF_OFF + (tt * 112 + ks0 + t) * 1024, v); });
    };
    if (nb < 8) {
        const float sc = nb < 4 ? QSCALE : 1.0f;
        const unsigned hs = nb < 4 ? 64 * 1024 : 64 * KVT;
        const unsigned base = nb < 4 ? QF_OFF + nb * 8 * hs + tt * 1024 : KV_OFF + (nb - 4) * 8 * hs + tt * KVT;
        sfor<0, 8>([&](auto HH) { constexpr int t = HH.value >> 1, o = (HH.value & 1) * 8;
            u32x4 v = {cvtpk(acc[t][o] * sc, acc[t][o + 1] * sc), cvtpk(acc[t][o + 2] * sc, acc[t][o + 3] * sc), cvtpk(acc[t][o + 4] * sc, acc[t][o + 5] * sc), cvtpk(acc[t][o + 6] * sc, acc[t][o + 7] * sc)};
            st16_wt(rs, lv, base + HH.value * hs, v); });
    } else if (nb < 12) {
        const unsigned base = KV_OFF + (nb - 8) * 8 * 64 * KVT + tt * KVT;
        const u32x4 one = {0x3f803f80u, 0x3f803f80u, 0x3f803f80u, 0x3f803f80u};
        st16_wt(rs, (l >> 3) * 64 * KVT + 2048 + (l & 7) * 16, base, one);
        const unsigned wa = xs + h * 512 + (((l & 31) ^ (4 * h)) * 16);
        const unsigned ta = xs + g * 1024 + (pp & 1) * 512 + (((8 * h + q) ^ (4 * (pp & 1))) * 16) + 8 * (pp >> 1);
        const unsigned so = g * 64 * KVT + 1024 + (h * 16 + (l & 15)) * 16;
        sfor<0, 4>([&](auto T) { constexpr int t = T.value, lo = (t & 1) * 2048;
            sfor<0, 2>([&](auto HH) { constexpr int o = HH.value * 8;
                u32x4 v = {cvtpk(acc[t][o], acc[t][o + 1]), cvtpk(acc[t][o + 2], acc[t][o + 3]), cvtpk(acc[t][o + 4], acc[t][o + 5]), cvtpk(acc[t][o + 6], acc[t][o + 7])};
                ldsw128(wa + lo + HH.value * 1024, v); });
            const u32x4 f0 = trfrag(ta + lo, (ta ^ 64) + lo), f1 = trfrag(ta + lo + 256, (ta ^ 64) + lo + 256);
            st16_wt(rs, so, base + t * 2 * 64 * KVT, f0); st16_wt(rs, so + 512, base + t * 2 * 64 * KVT, f1); });
    } else ffn_epi(IC<4>{}, (4 + nb) * 4);
    drain_bar();
    if (tid == 0) st_flag(p.ws + F1_OFF + nb * 128 + mb * 8, tag);
    FSTAMP(3);
    __builtin_amdgcn_s_setprio(GPRIO);
    { const rsrc_t gr = mkrs(p.gw, 1024);
      asm volatile("s_mov_b32 m0, %0\n\ts_nop 0\n\tbuffer_load_dwordx4 %1, %2, 0 offen lds" :: "s"(xs), "v"(lv), "s"(gr) : "memory"); }
    cur = ffn_stream(nb);
    sfor<0, 6>([&](auto S) { wdma<S.value * 8192>(cur, wdst); });
    waitv<12>();
    { f32x16 ga = {};
      sfor<0, 32>([&](auto S) { ga = MFMA(BF8(ldsr128(xs + S.value * 32 + h * 16)), BF8(xf[S.value]), ga); });
      const float gate = 1.0f / (1.0f + __expf(-ga[0]));
      *(LAS float*)(uintptr_t)(xs + 3072 + (l & 31) * 4) = gate; }
    FSTAMP(4);
    nxt = ffn_stream(20 + (nb >> 1));
    g1_pass<false, 4>(acc, xf, raf, cur, nxt, wdst, xw, xr, xv, xb);
    ffn_epi(IC<4>{}, nb * 4);
    FSTAMP(5);
    const int tt0 = mb * 4 + (nb & 1) * 2, n0 = (nb >> 1) * 64;
    cur.sa = mkrs(p.wd + (size_t)w * 448 * 1024 + n0 * 2, 0x7fffffffu); cur.sb = cur.sa;
    cur.va = (l >> 3) * 1024 + swz; cur.vb = cur.va + 8192 - 1024; cur.step = 16384; cur.so = 0u - 16384;
    ra[0] = (nb & 1) ? raf[2] : raf[0]; ra[1] = (nb & 1) ? raf[3] : raf[1];
    g1_pass<false, 2>(acc, xf, ra, nxt, cur, wdst, xw, xr, xv, xb);
    ffn_epi(IC<2>{}, (20 + (nb >> 1)) * 4 + (nb & 1) * 2);
    drain_bar();
    if (tid == 0) st_flag(p.ws + F2_OFF + mb * 128 + nb * 8, tag);
    FSTAMP(6);
    __builtin_amdgcn_s_setprio(0);
    wait_flags<4>(p.ws + F2_OFF + mb * 128 + (l & 15) * 8, tag);
    __builtin_amdgcn_s_setprio(GPRIO);
    FSTAMP(7);
    const rsrc_t ha = mkrs(p.ws + HF_OFF + ((size_t)tt0 * 112 + w * 28) * 1024, 28 * 1024), hb = mkrs(p.ws + HF_OFF + ((size_t)(tt0 + 1) * 112 + w * 28) * 1024, 28 * 1024);
    unsigned hso = 0;
    u32x4 hf[8][2];
    sfor<0, 7>([&](auto J) { hld2(hf[J.value][0], hf[J.value][1], lv, ha, hb, hso); });
    unsigned r2[2];
    for (int t = 0; t < 2; ++t) r2[t] = wdst + (4 * h + q) * 128 + pp * 8 + (((2 * t + g) ^ qh2) * 32);
    u32x4 wf0 = trfrag(r2[0], r2[0] + 1024), wf1 = trfrag(r2[1], r2[1] + 1024);
    f32x16 accB[4] = {};
    const rsrc_t wors = mkrs(p.wo + (size_t)w * 128 * 1024 + n0 * 2, 0x7fffffffu);
    for (int it = 0; it < 4; ++it) {
        sfor<0, 7>([&](auto J) {
            constexpr int j = J.value, on = ((j + 1) % 7) * 8192;
            waitv2<(12 + 2 * j < 18 ? 12 + 2 * j : 18)>(hf[j][0], hf[j][1]);
            accB[0] = MFMA(BF8(wf0), BF8(hf[j][0]), accB[0]); SBAR();
            if constexpr (j == 1) { if (it == 3) { cur.sa = wors; cur.so = 0u - 16384; } }
            cur.sb = cur.sa;
            wdma<((j + 6) % 7) * 8192>(cur, wdst); SBAR();
            accB[1] = MFMA(BF8(wf0), BF8(hf[j][1]), accB[1]); SBAR();
            wf0 = trfrag(r2[0] + on, r2[0] + on + 1024); SBAR();
            accB[2] = MFMA(BF8(wf1), BF8(hf[j][0]), accB[2]); SBAR();
            accB[3] = MFMA(BF8(wf1), BF8(hf[j][1]), accB[3]); SBAR();
            wf1 = trfrag(r2[1] + on, r2[1] + on + 1024);
            hld2(hf[j][0], hf[j][1], lv, ha, hb, hso); SBAR();
        });
    }
    FSTAMP(8);
    wdma<6 * 8192>(cur, wdst); wdma<7 * 8192>(cur, wdst);
    sfor<0, 7>([&](auto J) { waitv2<0>(hf[J.value][0], hf[J.value][1]); });
    drain_bar();
    const int wt = w & 1, wk = w >> 1;
    u32x4 wfo[16][2];
    { const unsigned d = wk * 4096 - w * 2048;
      sfor<0, 16>([&](auto M) { constexpr int on = (M.value & 7) * 8192 + (M.value >> 3) * 2048;
          wfo[M.value][0] = trfrag(r2[0] + d + on, r2[0] + d + on + 1024); wfo[M.value][1] = trfrag(r2[1] + d + on, r2[1] + d + on + 1024); }); }
    const float gate = *(const LAS float*)(uintptr_t)(ring + XS_OFF + ((nb & 1) * 2 + wt) * 4096 + 3072 + (l & 31) * 4);
    lds_bar();
    sfor<0, 4>([&](auto T) { constexpr int t = T.value;
        for (int r = 0; r < 4; ++r) {
            f32x4 v = {accB[t][4 * r], accB[t][4 * r + 1], accB[t][4 * r + 2], accB[t][4 * r + 3]};
            ldsw128(ring + ((t * 4 + w) * 4 + r) * 1024 + lv, __builtin_bit_cast(u32x4, v)); } });
    lds_bar();
    f32x16 own;
    for (int r = 0; r < 4; ++r) {
        f32x4 v = __builtin_bit_cast(f32x4, ldsr128(ring + ((w * 4) * 4 + r) * 1024 + lv));
        for (int o = 1; o < 4; ++o) v += __builtin_bit_cast(f32x4, ldsr128(ring + ((w * 4 + o) * 4 + r) * 1024 + lv));
        v *= 1.0f - gate;
        own[4 * r] = v[0]; own[4 * r + 1] = v[1]; own[4 * r + 2] = v[2]; own[4 * r + 3] = v[3]; }
    PIN(own);
    const rsrc_t oa = mkrs(p.ws + OF_OFF + ((size_t)(tt0 + wt) * 32 + wk * 16) * 1024, 8 * 1024), ob = mkrs(p.ws + OF_OFF + ((size_t)(tt0 + wt) * 32 + wk * 16 + 8) * 1024, 8 * 1024);
    char* orow = p.out + ((size_t)(tt0 + wt) * 32 + (l & 31)) * 1024 + (n0 + wk * 32) * 2 + h * 16;
    __builtin_amdgcn_s_setprio(0);
    wait_flags<1>(p.ws + F3_OFF + (tt0 >> 1) * 256 + (wk * 16 + (l & 15)) * 8, tag);
    __builtin_amdgcn_s_setprio(3);
    FSTAMP(9);
    { unsigned oso = 0;
      sfor<0, 8>([&](auto J) { hld2(hf[J.value][0], hf[J.value][1], lv, oa, ob, oso); }); }
    f32x16 accA[2];
    sfor<0, 8>([&](auto MM) { constexpr int m = MM.value;
        waitv2<14 - 2 * m>(hf[m][0], hf[m][1]);
        sfor<0, 2>([&](auto T) { constexpr int t = T.value;
            if constexpr (m == 0) accA[t] = MFMA(BF8(wfo[0][t]), BF8(hf[0][0]), f32x16{});
            else accA[t] = MFMA(BF8(wfo[m][t]), BF8(hf[m][0]), accA[t]);
            accA[t] = MFMA(BF8(wfo[m + 8][t]), BF8(hf[m][1]), accA[t]); });
        SBAR();
    });
    { const f32x16 snd = wk ? accA[0] : accA[1];
      for (int r = 0; r < 4; ++r) { f32x4 v = {snd[4 * r], snd[4 * r + 1], snd[4 * r + 2], snd[4 * r + 3]};
          ldsw128(ring + (w * 16 + r) * 1024 + lv, __builtin_bit_cast(u32x4, v)); } }
    { const f32x16 kp = wk ? accA[1] : accA[0];
      for (int r = 0; r < 16; ++r) own[r] += gate * kp[r]; }
    lds_bar();
    for (int r = 0; r < 4; ++r) {
        const f32x4 v = __builtin_bit_cast(f32x4, ldsr128(ring + ((w ^ 2) * 16 + r) * 1024 + lv));
        own[4 * r] += gate * v[0]; own[4 * r + 1] += gate * v[1]; own[4 * r + 2] += gate * v[2]; own[4 * r + 3] += gate * v[3]; }
    for (int i = 0; i < 4; i += 2) {
        unsigned a0 = cvtpk(own[4 * i], own[4 * i + 1]), a1 = cvtpk(own[4 * i + 2], own[4 * i + 3]);
        unsigned b0 = cvtpk(own[4 * i + 4], own[4 * i + 5]), b1 = cvtpk(own[4 * i + 6], own[4 * i + 7]);
        auto r0 = __builtin_amdgcn_permlane32_swap(a0, b0, false, false);
        auto r1 = __builtin_amdgcn_permlane32_swap(a1, b1, false, false);
        u32x4 v = {r0[0], r1[0], r0[1], r1[1]};
        *(u32x4*)(orow + i * 16) = v;
    }
    FSTAMP(10);
}

DI f32x16 smf(u32x4 a, f32x8 b, f32x16 c, int idx) {
    return __builtin_amdgcn_smfmac_f32_32x32x32_bf16(BF8(a), __builtin_bit_cast(bf16x16, b), c, idx, 0, 0);
}
DI f32x8 lo8(const f32x16& s) { return __builtin_shufflevector(s, s, 0, 1, 2, 3, 4, 5, 6, 7); }
DI f32x8 hi8(const f32x16& s) { return __builtin_shufflevector(s, s, 8, 9, 10, 11, 12, 13, 14, 15); }
template <int B, int E> DI void exps(f32x16& s) {
#pragma unroll
    for (int i = B; i < E; ++i) { float t = __builtin_amdgcn_exp2f(s[i]); PIN(t); s[i] = t; }
}
DI float rowmax16(const f32x16& s) {
    float a = __builtin_fmaxf(s[0], s[1]);
#pragma unroll
    for (int i = 2; i < 16; ++i) a = __builtin_fmaxf(a, s[i]);
    return a;
}

DI void a_role(const Params& p, int ai, char* lds) {
    const int tid = threadIdx.x, l = tid & 63, w = __builtin_amdgcn_readfirstlane(tid >> 6);
    const int head = (ai & 7) * 4 + ((ai >> 3) & 3), qb = ai >> 5, qh = w & 1, kh = w >> 1;
    const unsigned base = (unsigned)(uintptr_t)lds;
    const rsrc_t rs = mkrs(p.ws, WS_USED);
    const rsrc_t rkv = mkrs(p.ws + KV_OFF + (size_t)(head * 64 + kh * 32) * KVT, 32 * KVT);
    const unsigned lv = l * 16;
    FSTAMP(0);
    const u64 epoch = rfl64(ld_flag(p.ws + FL_OFF));
    const u64 tag = mix64(epoch);
    drain_bar();
    if (tid == 0) st_flag(p.ws + F0_OFF + ai * 8, tag);
    auto bump = [&]() {
        if (ai == 0 && w == 0) {
            const u32x2 t2 = __builtin_bit_cast(u32x2, tag);
#pragma nounroll
            for (unsigned spins = 0; spins < SPIN_CAP; ++spins) {
                unsigned d = 0;
                for (int i = 0; i < 4; ++i) { const u32x4 f = ld16_dev(rs, lv, F1_OFF + i * 1024); d |= (f[0] ^ t2[0]) | (f[1] ^ t2[1]) | (f[2] ^ t2[0]) | (f[3] ^ t2[1]); }
                if (__all(d == 0)) break;
                asm volatile("s_sleep 8" ::: "memory");
            }
            if (l == 0) st_flag(p.ws + FL_OFF, epoch + 1);
        } };
    const int qt = qb * 8 + qh * 4;
    const int g = (l >> 4) & 1;
    const unsigned oo = ((l & 11) == 0) ? 2048u : 0x7ff00000u;
    const unsigned v0o = g ? oo : 1024u + ((l >> 5) * 16 + (l & 15)) * 16, v1o = g ? oo : v0o + 512;
    int idx = 0xDDDD; PIN(idx);
    f32x16 o[4] = {};
    for (int i = 0; i < 4; ++i) PIN(o[i]);
    __builtin_amdgcn_s_sleep(64);
    __builtin_amdgcn_s_setprio(APRIO);
    { const int j = (l & 16) ? (head >> 3) : ((l & 8) ? 8 + (head >> 3) : 4 + (head >> 3)), m = (l & 16) ? qb * 2 + qh : kh * 8 + (l & 7);
      wait_flags<2>(p.ws + F1_OFF + j * 128 + m * 8, tag); }
    FSTAMP(1);
    u32x4 qf[4];
    int nmax = 1;
    for (int attempt = 0; attempt < 2; ++attempt) {
        unsigned so = qb * 4 * KVT;
        u32x4 kA = ld16_dev(rkv, lv, so);
        for (int i = 0; i < 4; ++i) qf[i] = ld16_dev(rs, lv, QF_OFF + (head * 64 + qt + i) * 1024);
        SBAR();
        u32x4 v0A = ld16_dev(rkv, v0o, so), v1A = ld16_dev(rkv, v1o, so);
        SBAR();
        u32x4 kB = ld16_dev(rkv, lv + KVT, so);
        SBAR();
        u32x4 v0B = ld16_dev(rkv, v0o + KVT, so), v1B = {};
        SBAR();
        float mx[4];
        for (int i = 0; i < 4; ++i) mx[i] = rowmax16(MFMA(BF8(kA), BF8(qf[i]), f32x16{}));
        for (int t = 0; t < nmax - 1; ++t) {
            const u32x4 kf = ld16_dev(rkv, lv, t * KVT);
            for (int i = 0; i < 4; ++i) mx[i] = __builtin_fmaxf(mx[i], rowmax16(MFMA(BF8(kf), BF8(qf[i]), f32x16{})));
        }
        f32x16 c[4];
        for (int i = 0; i < 4; ++i) {
            auto rr = __builtin_amdgcn_permlane32_swap(__float_as_uint(mx[i]), __float_as_uint(mx[i]), false, false);
            mx[i] = __builtin_fmaxf(__uint_as_float(rr[0]), __uint_as_float(rr[1]));
            for (int r = 0; r < 16; ++r) c[i][r] = -mx[i];
            PIN(c[i]); }
        f32x16 sc = MFMA(BF8(kA), BF8(qf[0]), c[0]), sn;
        f32x8 ph = {};
        FSTAMP(2);
        auto tile = [&](u32x4& K, u32x4& V0, u32x4& V1, u32x4& Kn, u32x4& V1p, unsigned o1, unsigned o2, auto ODD) {
            constexpr unsigned a1 = ODD.value ? 0 : KVT, a2 = ODD.value ? KVT : 0;
            sfor<0, 4>([&](auto I) {
                constexpr int i = I.value, in = (i + 1) & 3, ip = (i + 3) & 3;
                SBAR();
                sn = MFMA(BF8(i < 3 ? K : Kn), BF8(qf[in]), c[in]); SBAR();
                if constexpr (i == 2) { K = ld16_dev(rkv, lv + a2, o2); SBAR(); }
                exps<0, 5>(sc); SBAR();
                o[ip] = smf(i == 0 ? V1p : V1, ph, o[ip], idx); SBAR();
                if constexpr (i == 0) { V1p = ld16_dev(rkv, v1o + a1, o1); SBAR(); }
                exps<5, 8>(sc); SBAR();
                exps<8, 11>(sc); SBAR();
                o[i] = smf(V0, lo8(sc), o[i], idx); SBAR();
                if constexpr (i == 3) { V0 = ld16_dev(rkv, v0o + a2, o2); SBAR(); }
                exps<11, 16>(sc); SBAR();
                ph = hi8(sc); sc = sn;
            });
        };
        for (int t = 0; t < 16; ++t) {
            unsigned sn2 = so + 2 * KVT; if (sn2 == 32 * KVT) sn2 = 0;
            tile(kA, v0A, v1A, kB, v1B, so, sn2, IC<0>{});
            tile(kB, v0B, v1B, kA, v1A, sn2, sn2, IC<1>{});
            so = sn2;
        }
        o[3] = smf(v1B, ph, o[3], idx);
        FSTAMP(3);
        sfor<0, 2>([&](auto U) { constexpr int u = U.value;
            const f32x16& s = kh ? o[u] : o[2 + u]; const float m = kh ? mx[u] : mx[2 + u];
            const unsigned a = base + (w * 2 + u) * 3072 + lv;
            f32x4 x0 = {s[0], s[1], s[2], s[3]}, x1 = {s[4], s[5], s[6], s[7]}, x2 = {s[8], m, 0.f, 0.f};
            ldsw128(a, __builtin_bit_cast(u32x4, x0)); ldsw128(a + 1024, __builtin_bit_cast(u32x4, x1)); ldsw128(a + 2048, __builtin_bit_cast(u32x4, x2)); });
        float big = 0.f;
        for (int i = 0; i < 4; ++i) { big += o[i][8] * 1.0e19f;
            for (int r = 0; r < 8; ++r) big += __builtin_fabsf(o[i][r]); }
        *(LAS int*)(uintptr_t)(base + 32768 + w * 4) = __any(!(big < 1.0e37f)) ? 1 : 0;
        lds_bar();
        const u32x4 bw = ldsr128(base + 32768);
        const int redo = attempt == 0 ? (int)(bw[0] | bw[1] | bw[2] | bw[3]) : 0;
        if (!__builtin_amdgcn_readfirstlane(redo)) {
            sfor<0, 2>([&](auto U) { constexpr int u = U.value;
                const f32x16& s = kh ? o[2 + u] : o[u]; const float m = kh ? mx[2 + u] : mx[u];
                const unsigned a = base + ((w ^ 2) * 2 + u) * 3072 + lv;
                const f32x4 x0 = __builtin_bit_cast(f32x4, ldsr128(a)), x1 = __builtin_bit_cast(f32x4, ldsr128(a + 1024)), x2 = __builtin_bit_cast(f32x4, ldsr128(a + 2048));
                const float mm = __builtin_fmaxf(m, x2[1]);
                float fa = __builtin_amdgcn_exp2f(m - mm), fb = __builtin_amdgcn_exp2f(x2[1] - mm);
                const float r = __builtin_amdgcn_rcpf(s[8] * fa + x2[0] * fb);
                fa *= r; fb *= r;
                u32x4 v = {cvtpk(s[0] * fa + x0[0] * fb, s[1] * fa + x0[1] * fb), cvtpk(s[2] * fa + x0[2] * fb, s[3] * fa + x0[3] * fb),
                           cvtpk(s[4] * fa + x1[0] * fb, s[5] * fa + x1[1] * fb), cvtpk(s[6] * fa + x1[2] * fb, s[7] * fa + x1[3] * fb)};
                st16_wt(rs, lv, OF_OFF + ((qt + kh * 2 + u) * 32 + head) * 1024, v); });
            break;
        }
        lds_bar();
        nmax = 33;
        for (int i = 0; i < 4; ++i) o[i] = f32x16{};
    }
    waitv<0>();
    if (l == 0) st_flag(p.ws + F3_OFF + ((qt >> 1) + kh) * 256 + head * 8, tag);
    FSTAMP(4);
    bump();
}

__global__ __launch_bounds__(256, 2) void k_fused(Params p) {
    extern __shared__ __attribute__((aligned(16))) char lds[];
    if (blockIdx.x < 256) g_role(p, blockIdx.x, lds);
    else a_role(p, blockIdx.x - 256, lds);
}

extern "C" void kernel_launch(void* const* d_in, const int* in_sizes, int n_in, void* d_out, int out_size, void* d_ws, size_t ws_size,
                              hipStream_t stream) {
    Params p;
    p.x = (const char*)d_in[0]; p.wqkv = (const char*)d_in[1]; p.wo = (const char*)d_in[2];
    p.wg = (const char*)d_in[4]; p.wu = (const char*)d_in[5]; p.wd = (const char*)d_in[6]; p.gw = (const char*)d_in[7];
    p.out = (char*)d_out; p.ws = (char*)d_ws;
    (void)hipFuncSetAttribute((const void*)k_fused, hipFuncAttributeMaxDynamicSharedMemorySize, LDS_BYTES);
    k_fused<<<512, 256, LDS_BYTES, stream>>>(p);
}
```

```cpp
#include <hip/hip_runtime.h>
#include <hip/hip_bf16.h>
#include <stdint.h>
#include <type_traits>

typedef __bf16 bf16x8 __attribute__((ext_vector_type(8)));
typedef __bf16 bf16x16 __attribute__((ext_vector_type(16)));
typedef float f32x16 __attribute__((ext_vector_type(16)));
typedef float f32x8 __attribute__((ext_vector_type(8)));
typedef float f32x4 __attribute__((ext_vector_type(4)));
typedef unsigned u32x4 __attribute__((ext_vector_type(4)));
typedef unsigned u32x2 __attribute__((ext_vector_type(2)));
typedef unsigned long long u64;
typedef __amdgpu_buffer_rsrc_t rsrc_t;
#define LAS __attribute__((address_space(3)))
#define GAS __attribute__((address_space(1)))
#define DI __device__ __forceinline__

constexpr unsigned QF_OFF = 0;
constexpr unsigned KVT = 2176;
constexpr unsigned KV_OFF = 2u << 20;
constexpr unsigned HF_OFF = 7u << 20;
constexpr unsigned OF_OFF = 14u << 20;
constexpr unsigned FL_OFF = 16u << 20;
constexpr unsigned F0_OFF = FL_OFF + 4096;
constexpr unsigned F3_OFF = FL_OFF + 8192;
constexpr unsigned F1_OFF = FL_OFF + 16384;
constexpr unsigned F2_OFF = FL_OFF + 28672;
constexpr unsigned GT_OFF = FL_OFF + 32768;
constexpr unsigned WS_USED = 17u << 20;
constexpr int LDS_BYTES = 81920;
constexpr int XS_OFF = 65536;
constexpr float QSCALE = 0.25f * 1.4426950408889634f;
constexpr unsigned SPIN_CAP = 1u << 18;
#define APRIO 2
#define GPRIO 0

struct Params {
    const char* x; const char* wqkv; const char* wo; const char* wg; const char* wu; const char* wd; const char* gw;
    char* out; char* ws;
};

template <int I> using IC = std::integral_constant<int, I>;
template <int B, int E, class F> DI void sfor(F&& f) { if constexpr (B < E) { f(IC<B>{}); sfor<B + 1, E>(f); } }

#define FSTAMP(i)
#define SLOTSTAMP(i)
#define SLOTDECL
#define SLOTOUT(m)
#define MFMA(a, b, c) __builtin_amdgcn_mfma_f32_32x32x16_bf16((a), (b), (c), 0, 0, 0)
#define SBAR() __builtin_amdgcn_sched_barrier(0)
#define PIN(x) asm volatile("" : "+v"(x))
#define RLX_AGENT __ATOMIC_RELAXED, __HIP_MEMORY_SCOPE_AGENT
#define BF8(x) __builtin_bit_cast(bf16x8, (x))

DI unsigned rfl(unsigned v) { return (unsigned)__builtin_amdgcn_readfirstlane((int)v); }
DI u64 rfl64(u64 v) { return ((u64)(unsigned)__builtin_amdgcn_readfirstlane((int)(v >> 32)) << 32) | (unsigned)__builtin_amdgcn_readfirstlane((int)v); }
DI rsrc_t mkrs(const char* p, unsigned bytes) { return __builtin_amdgcn_make_buffer_rsrc((void*)rfl64((u64)p), 0, (int)bytes, 0x00020000); }

struct WStream { rsrc_t sa, sb; unsigned va, vb, so, step, step2, wrap; };
template <int DST> DI void wdma(WStream& s, unsigned wdst) {
    asm volatile("s_add_u32 m0, %[wd], %[imm]\n\ts_add_u32 %[so], %[so], %[st]\n\ts_cmp_lg_u32 %[so], %[wr]\n\ts_cselect_b32 %[so], %[so], 0\n\t"
                 "buffer_load_dwordx4 %[va], %[sa], %[so] offen lds\n\tbuffer_load_dwordx4 %[vb], %[sb], %[so] offen offset:1024 lds"
                 : [so] "+s"(s.so) : [wd] "s"(wdst), [imm] "n"(DST), [st] "s"(s.step), [wr] "s"(s.wrap), [va] "v"(s.va), [vb] "v"(s.vb), [sa] "s"(s.sa), [sb] "s"(s.sb)
                 : "scc", "memory");
}
#define ROTM 0
template <int DST, int PIECE> DI void wdma1(WStream& s, unsigned wdst) {
    if constexpr (PIECE == 0 && ROTM != 0 && (DST & 8192) == 0)
        asm volatile("s_add_u32 m0, %[wd], %[imm]\n\ts_add_u32 %[so], %[so], %[st]\n\ts_cmp_lg_u32 %[so], %[wr]\n\ts_cselect_b32 %[so], %[so], 0\n\t"
                     "buffer_load_dwordx4 %[va], %[sa], %[so] offen lds"
                     : [so] "+s"(s.so) : [wd] "s"(wdst), [imm] "n"(DST), [st] "s"(s.step2), [wr] "s"(s.wrap), [va] "v"(s.va), [sa] "s"(s.sa) : "scc", "memory");
    else if constexpr (PIECE == 0)
        asm volatile("s_add_u32 m0, %[wd], %[imm]\n\ts_add_u32 %[so], %[so], %[st]\n\tbuffer_load_dwordx4 %[va], %[sa], %[so] offen lds"
                     : [so] "+s"(s.so) : [wd] "s"(wdst), [imm] "n"(DST), [st] "s"((DST & 8192) ? s.step : s.step2), [va] "v"(s.va), [sa] "s"(s.sa) : "scc", "memory");
    else
        asm volatile("buffer_load_dwordx4 %[vb], %[sb], %[so] offen offset:1024 lds" :: [so] "s"(s.so), [vb] "v"(s.vb), [sb] "s"(s.sb) : "memory");
}
template <int OFF> DI u32x4 xld16(unsigned voff, rsrc_t rs, unsigned so) {
    u32x4 r; asm volatile("buffer_load_dwordx4 %0, %1, %2, %3 offen offset:%4" : "=v"(r) : "v"(voff), "s"(rs), "s"(so), "n"(OFF) : "memory"); return r;
}
template <int X, int D> DI void qsel2(u32x4& r, const u32x4& a, const u32x4& b, u64 keep) {
    unsigned r0, r1; const unsigned a0 = a[D], a1 = a[D + 1], b0 = b[D], b1 = b[D + 1];
    register u64 kv asm("vcc") = keep;
    if constexpr (X == 1)
        asm volatile("v_cndmask_b32_dpp %0, %2, %4, vcc quad_perm:[1,0,3,2] row_mask:0xf bank_mask:0xf\n\tv_cndmask_b32_dpp %1, %3, %5, vcc quad_perm:[1,0,3,2] row_mask:0xf bank_mask:0xf"
                     : "=&v"(r0), "=v"(r1) : "v"(a0), "v"(a1), "v"(b0), "v"(b1), "s"(kv));
    else
        asm volatile("v_cndmask_b32_dpp %0, %2, %4, vcc quad_perm:[2,3,0,1] row_mask:0xf bank_mask:0xf\n\tv_cndmask_b32_dpp %1, %3, %5, vcc quad_perm:[2,3,0,1] row_mask:0xf bank_mask:0xf"
                     : "=&v"(r0), "=v"(r1) : "v"(a0), "v"(a1), "v"(b0), "v"(b1), "s"(kv));
    r[D] = r0; r[D + 1] = r1;
}
DI void hld2(u32x4& a, u32x4& b, unsigned voff, rsrc_t sa, rsrc_t sb, unsigned& so) {
    asm volatile("buffer_load_dwordx4 %0, %3, %4, %2 offen sc1\n\tbuffer_load_dwordx4 %1, %3, %5, %2 offen sc1"
                 : "=&v"(a), "=&v"(b) : "s"(so), "v"(voff), "s"(sa), "s"(sb) : "memory");
    so += 1024;
}
template <int N> DI void waitv() { asm volatile("s_waitcnt vmcnt(%0)" ::"n"(N) : "memory"); }
template <int N> DI void waitv2(u32x4& a, u32x4& b) { asm volatile("s_waitcnt vmcnt(%2)" : "+v"(a), "+v"(b) : "n"(N) : "memory"); }
template <int N> DI void waitv_bar() { asm volatile("s_waitcnt vmcnt(%0)\n\ts_barrier" ::"n"(N) : "memory"); }
DI void lds_bar() { asm volatile("s_waitcnt lgkmcnt(0)\n\ts_barrier" ::: "memory"); }
DI void drain_bar() { asm volatile("s_waitcnt vmcnt(0) lgkmcnt(0)\n\ts_barrier" ::: "memory"); }

DI unsigned cvtpk(float lo, float hi) {
    typedef __bf16 b2 __attribute__((ext_vector_type(2)));
    b2 r; r[0] = (__bf16)lo; r[1] = (__bf16)hi; return __builtin_bit_cast(unsigned, r);
}
DI u32x2 trread(unsigned addr) {
    typedef short v4i16_t __attribute__((ext_vector_type(4)));
    return __builtin_bit_cast(u32x2, __builtin_amdgcn_ds_read_tr16_b64_v4i16((LAS v4i16_t*)(uintptr_t)addr));
}
DI u32x4 trfrag(unsigned a0, unsigned a1) { u32x2 lo = trread(a0), hi = trread(a1); u32x4 r = {lo[0], lo[1], hi[0], hi[1]}; return r; }
DI u32x4 ldsr128(unsigned addr) { return *(const LAS u32x4*)(uintptr_t)addr; }
DI void ldsw128(unsigned addr, u32x4 v) { *(LAS u32x4*)(uintptr_t)addr = v; }
DI void st16_wt(rsrc_t rs, unsigned voff, unsigned soff, u32x4 v) {
    __builtin_amdgcn_raw_buffer_store_b128(v, rs, voff, soff, 16);
    asm volatile("s_nop 1" :: "v"(v) : "memory");
}
DI u32x4 ld16_dev(rsrc_t rs, unsigned voff, unsigned soff) { return __builtin_amdgcn_raw_buffer_load_b128(rs, voff, soff, 16); }

DI u64 mix64(u64 z) { z += 0x9E3779B97F4A7C15ull; z = (z ^ (z >> 30)) * 0xBF58476D1CE4E5B9ull; z = (z ^ (z >> 27)) * 0x94D049BB133111EBull; return z ^ (z >> 31); }
DI u64 ld_flag(const char* a) { return __hip_atomic_load((const GAS u64*)a, RLX_AGENT); }
constexpr unsigned OOR = 0x7ff00000u;
DI unsigned sel(bool c, unsigned a, unsigned b) { const unsigned m = 0u - (unsigned)c; return (a & m) | (b & ~m); }
DI void st_flag(rsrc_t rs, unsigned voff, u64 v) { __builtin_amdgcn_raw_buffer_store_b64(__builtin_bit_cast(u32x2, v), rs, voff, 0, 16); }
template <int SLEEP> DI void wait_flags(const char* a, u64 tag) {
#pragma nounroll
    for (unsigned spins = 0; spins < SPIN_CAP; ++spins) {
        if (__all(ld_flag(a) == tag)) break;
        __builtin_amdgcn_s_sleep(SLEEP);
    }
    asm volatile("" ::: "memory");
}

#define NPRO 3
#define XSTEP 2
#define NPRE 4
#define PUBS 3
constexpr int XW0 = 5;
constexpr int AHEAD = 6;
struct Op { int x, a, b, gap; };
struct Plan { Op op[96]; int n; };
constexpr int bar_gap(int s, int NT) { return s <= 0 ? -1 : (2 * s - 1) * NT; }
constexpr Plan mk_plan(int mode, int NT) {
    Plan P{};
    auto put = [&](int x, int a, int b, int gap) { P.op[P.n] = Op{x, a, b, gap}; ++P.n; };
    int ws = 0, wp = 0, xc = mode == 0 ? 0 : 8, xj = 0, g = 0;
    const int wend = 16 + (mode == 0 ? NPRE : AHEAD);
    if (mode == 0) for (int i = 0; i < NPRO; ++i) {
        for (int j = 0; j < 4; ++j) put(1, i, j, -1);
        for (int j = 0; j < 4; ++j) put(0, 2 * i + (j >> 1), j & 1, -1);
        xc = i + 1; ws = 2 * i + 2; }
    if (mode == 1) { ws = NPRE; g = bar_gap(PUBS, NT); }
    if (mode == 2) { for (int j = 0; j < 2 * AHEAD; ++j) put(0, j >> 1, j & 1, -2); ws = AHEAD; }
    while (g < 32 * NT && (ws < wend || xc < 8)) {
        const int e = (mode == 0 && ws < 8) ? -1 : bar_gap(ws - AHEAD, NT);
        const bool wok = ws < wend && g >= e + (wp ? NT : 0) + 1, behind = ws < wend && g > bar_gap(ws + 1 - AHEAD, NT);
        if (xc < 8 && (2 * xc <= ws || !wok)) { put(1, xc, xj, g); if (++xj == 4) { xj = 0; ++xc; } g += XSTEP; }
        else if (wok) { put(0, ws, wp, g); if (++wp == 2) { wp = 0; ++ws; } g += (mode == 0 && xc < 8) ? XSTEP : (behind ? 1 : 1); }
        else ++g;
    }
    return P;
}
constexpr int plan_wait(const Plan& P, int gap, int x, int a, int b) {
    int issued = 0, last = -1;
    for (int i = 0; i < P.n; ++i) { if (P.op[i].gap < gap || P.op[i].gap < 0) ++issued; if (P.op[i].x == x && P.op[i].a == a && P.op[i].b == b) last = i; }
    return last < 0 ? -1 : issued - last - 1;
}
constexpr int plan_first(const Plan& P, int gap) { int i = 0; while (i < P.n && P.op[i].gap < gap) ++i; return i; }
template <int MODE, int NT> struct PlanOf { static constexpr Plan P = mk_plan(MODE, NT); };

template <int MODE, int NT, class PUB>
DI void g1_pass(f32x16 (&acc)[4], u32x4 (&xf)[32], const unsigned (&ra_)[4], WStream& cur, WStream& nxt, unsigned wdst,
                unsigned xv, rsrc_t xrs, unsigned rot, PUB&& pub, char* wsdbg) {
    SLOTDECL;
    using PL = PlanOf<MODE, NT>;
    constexpr bool FIRST = MODE == 0;
    u32x4 wf[2][4];
    unsigned ra[4];
    auto issue = [&](auto GG) {
        sfor<plan_first(PL::P, GG.value), plan_first(PL::P, GG.value + 1)>([&](auto I) {
            constexpr Op o = PL::P.op[I.value];
            if constexpr (o.x) xf[4 * o.a + o.b] = xld16<((o.b >> 1) + (o.b & 1) * 2) * 1024>(xv, xrs, ((o.a + rot) & 7) * 128);
            else if constexpr (o.a < 16) wdma1<(o.a & 7) * 8192, o.b>(cur, wdst);
            else wdma1<(o.a & 7) * 8192, o.b>(nxt, wdst); });
    };
    issue(IC<-1>{});
    sfor<0, NT>([&](auto T) { ra[T.value] = ra_[T.value]; PIN(ra[T.value]); });
    u32x4 tq[4];
    auto xtr = [&](auto CC, auto MM, auto GG) {
        constexpr int c = CC.value, g = MM.value >> 1, d = (MM.value & 1) * 2;
        if constexpr (MM.value == 0 || MM.value == 4) {
            constexpr int nw = plan_wait(PL::P, GG.value, 1, c, g + 1);
            static_assert(nw >= 0 && nw < 64, "x chunk issued too late");
            waitv2<nw>(xf[4 * c + g], xf[4 * c + g + 1]);
        }
        if constexpr (g < 4) qsel2<2, d>(tq[g], xf[4 * c + (g ^ 1)], xf[4 * c + g], (g & 1) ? 0xCCCCCCCCCCCCCCCCull : 0x3333333333333333ull);
        else { constexpr int i = g - 4, u = (i >> 1) + (i & 1) * 2;
               qsel2<1, d>(xf[4 * c + i], tq[u ^ 2], tq[u], (i & 1) ? 0xAAAAAAAAAAAAAAAAull : 0x5555555555555555ull); }
    };
    auto rd = [&](auto NN, auto T) {
        constexpr int n = NN.value, off = ((n >> 1) & 7) * 8192 + (n & 1) * 4096;
        wf[n & 1][T.value] = trfrag(ra[T.value] + off, ra[T.value] + off + 512);
    };
    auto bar = [&](auto SS) {
        constexpr int s = SS.value, nw = plan_wait(PL::P, bar_gap(s, NT), 0, s, 1);
        if constexpr (MODE == 1 && s == PUBS) { waitv_bar<0>(); pub(); }
        else if constexpr (MODE == 1 && s < NPRE) { if constexpr (s == 0 || s > PUBS) asm volatile("s_barrier" ::: "memory"); }
        else { static_assert(nw >= 0 && nw < 64, "slot issued too late"); waitv_bar<nw>(); }
        SLOTSTAMP(MODE * 16 + s);
    };
    if constexpr (FIRST) sfor<0, 16 - XW0>([&](auto M) { xtr(IC<0>{}, M, IC<-1>{}); });
    bar(IC<0>{});
    sfor<0, NT>([&](auto T) { rd(IC<0>{}, T); });
    SBAR();
    sfor<0, 32>([&](auto NN) {
        constexpr int n = NN.value;
        sfor<0, NT>([&](auto T) {
            constexpr int t = T.value;
            if constexpr (n == 0) acc[t] = MFMA(BF8(wf[0][t]), BF8(xf[0]), f32x16{});
            else acc[t] = MFMA(BF8(wf[n & 1][t]), BF8(xf[n]), acc[t]);
            SBAR();
            if constexpr (n + 1 < 32) {
                if constexpr ((n & 1) == 1 && t == 0) bar(IC<(n + 1) / 2 + t>{});
                rd(IC<n + 1>{}, T);
            }
            constexpr int gx = n * 4 + t + 16 - XW0;
            if constexpr (FIRST && gx < 128) xtr(IC<gx / 16>{}, IC<gx % 16>{}, IC<n * NT + t>{});
            issue(IC<n * NT + t>{});
            SBAR();
        });
    });
    SLOTOUT(MODE);
}

DI void g_role(const Params& p, int gi, char* lds) {
    const int tid = threadIdx.x, l = tid & 63, w = __builtin_amdgcn_readfirstlane(tid >> 6);
    const int h = l >> 5, g = (l >> 4) & 1, q = (l >> 2) & 3, pp = l & 3;
    const int mb = ((gi & 7) >> 1) * 4 + ((gi >> 3) & 3), nb = (gi >> 5) * 2 + (gi & 1);
    const unsigned ring = (unsigned)(uintptr_t)lds, wdst = ring + w * 2048;
    const int tt = mb * 4 + w;
    FSTAMP(0);
    const bool hasq = nb < 12;
    const unsigned swz = (((l >> 1) & 3) ^ (2 * ((l >> 4) & 1))) * 32 + (l & 1) * 16;
#define ROTM2 3
    const unsigned rot = ((gi >> 5) + 2 * (gi >> 3)) & ROTM;
    const unsigned wrow = 8 * (w >> 1) + 32 * (w & 1) + (l >> 3);
    auto ffn_stream = [&](int gq) { WStream s;
        s.sa = mkrs(p.wg + gq * 128, 0x7fffffffu); s.sb = mkrs(p.wu + gq * 128 - 1024, 0x7fffffffu);
        s.va = wrow * 3584 + swz; s.vb = s.va ^ 32; s.step = 16 * 3584; s.step2 = 48 * 3584; s.so = rfl((4 * rot - 3) * s.step); s.wrap = 512 * 3584; return s; };
    WStream cur, nxt;
    if (hasq) { cur.sa = mkrs(p.wqkv + nb * 256, 0x7fffffffu); cur.sb = mkrs(p.wqkv + nb * 256 + 128 - 1024, 0x7fffffffu);
        cur.va = wrow * 3072 + swz; cur.vb = cur.va ^ 32; cur.step = 16 * 3072; cur.step2 = 48 * 3072; cur.so = rfl((4 * rot - 3) * cur.step); cur.wrap = 512 * 3072; }
    else cur = ffn_stream(4 + nb);
    u32x4 xf[32];
    const rsrc_t xrs = mkrs(p.x + (size_t)tt * 32 * 1024, 32 * 1024); const unsigned xv = ((l >> 2) & 7) * 4096 + h * 64 + pp * 16;
    u64 epv;
    asm volatile("global_load_dwordx2 %0, %1, %2 sc1" : "=v"(epv) : "v"(0u), "s"(p.ws + FL_OFF) : "memory");
    nxt = ffn_stream(nb);
    const unsigned xs = ring + XS_OFF + w * 4096;
    const unsigned rbase = ring + h * 2048 + q * 128 + pp * 8;
    const int qh2 = (q >> 1) * 2;
    const rsrc_t rs = mkrs(p.ws, WS_USED);
    const unsigned lv = l * 16;
    f32x16 acc[4];
    unsigned ra[4], raf[4];
    for (int t = 0; t < 4; ++t) {
        raf[t] = rbase + g * 1024 + ((t ^ qh2 ^ g) * 32);
        ra[t] = hasq ? rbase + (t >> 1) * 1024 + (((2 * (t & 1) + g) ^ qh2 ^ (t >> 1)) * 32) : raf[t];
    }
    FSTAMP(1);
    auto nopub = []() {};
    g1_pass<0, 4>(acc, xf, ra, cur, nxt, wdst, xv, xrs, rot, nopub, p.ws);
    FSTAMP(2);
    waitv<0>();
    PIN(epv);
    const u64 tag = mix64(rfl64(epv));
    auto ffn_epi = [&](auto NT, int ks0) {
        sfor<0, NT.value>([&](auto T) { constexpr int t = T.value;
            u32x4 v = {cvtpk(acc[t][0] * acc[t][8], acc[t][1] * acc[t][9]), cvtpk(acc[t][2] * acc[t][10], acc[t][3] * acc[t][11]),
                       cvtpk(acc[t][4] * acc[t][12], acc[t][5] * acc[t][13]), cvtpk(acc[t][6] * acc[t][14], acc[t][7] * acc[t][15])};
            st16_wt(rs, lv, HF_OFF + (tt * 112 + ks0 + t) * 1024, v); });
    };
    if (nb < 8) {
        const float sc = nb < 4 ? QSCALE : 1.0f;
        const unsigned hs = nb < 4 ? 64 * 1024 : 64 * KVT;
        const unsigned base = nb < 4 ? QF_OFF + nb * 8 * hs + tt * 1024 : KV_OFF + (nb - 4) * 8 * hs + tt * KVT;
        sfor<0, 8>([&](auto HH) { constexpr int t = HH.value >> 1, o = (HH.value & 1) * 8;
            u32x4 v = {cvtpk(acc[t][o] * sc, acc[t][o + 1] * sc), cvtpk(acc[t][o + 2] * sc, acc[t][o + 3] * sc), cvtpk(acc[t][o + 4] * sc, acc[t][o + 5] * sc), cvtpk(acc[t][o + 6] * sc, acc[t][o + 7] * sc)};
            st16_wt(rs, lv, base + HH.value * hs, v); });
    } else if (nb < 12) {
        const unsigned base = KV_OFF + (nb - 8) * 8 * 64 * KVT + tt * KVT;
        const u32x4 one = {0x3f803f80u, 0x3f803f80u, 0x3f803f80u, 0x3f803f80u};
        st16_wt(rs, (l >> 3) * 64 * KVT + 2048 + (l & 7) * 16, base, one);
        const unsigned wa = xs + h * 512 + (((l & 31) ^ (4 * h)) * 16);
        const unsigned ta = xs + g * 1024 + (pp & 1) * 512 + (((8 * h + q) ^ (4 * (pp & 1))) * 16) + 8 * (pp >> 1);
        const unsigned so = g * 64 * KVT + 1024 + (h * 16 + (l & 15)) * 16;
        sfor<0, 4>([&](auto T) { constexpr int t = T.value, lo = (t & 1) * 2048;
            sfor<0, 2>([&](auto HH) { constexpr int o = HH.value * 8;
                u32x4 v = {cvtpk(acc[t][o], acc[t][o + 1]), cvtpk(acc[t][o + 2], acc[t][o + 3]), cvtpk(acc[t][o + 4], acc[t][o + 5]), cvtpk(acc[t][o + 6], acc[t][o + 7])};
                ldsw128(wa + lo + HH.value * 1024, v); });
            const u32x4 f0 = trfrag(ta + lo, (ta ^ 64) + lo), f1 = trfrag(ta + lo + 256, (ta ^ 64) + lo + 256);
            st16_wt(rs, so, base + t * 2 * 64 * KVT, f0); st16_wt(rs, so + 512, base + t * 2 * 64 * KVT, f1); });
    } else ffn_epi(IC<4>{}, (4 + nb) * 4);
    FSTAMP(3);
    __builtin_amdgcn_s_setprio(GPRIO);
    const unsigned f1o = sel(tid < (hasq ? 8 : 0), F1_OFF + ((nb & 3) * 8 + l) * 384 + (nb >> 2) * 128 + mb * 8, sel(tid == 8, F0_OFF + gi * 8, OOR));
    auto pub1 = [&]() { st_flag(rs, f1o, tag); FSTAMP(4); };
    cur = ffn_stream(20 + (nb >> 1));
    g1_pass<1, 4>(acc, xf, raf, nxt, cur, wdst, xv, xrs, rot, pub1, p.ws);
    ffn_epi(IC<4>{}, nb * 4);
    FSTAMP(5);
    const int tt0 = mb * 4 + (nb & 1) * 2, n0 = (nb >> 1) * 64;
    const unsigned rot2 = ((gi >> 5) + (gi >> 3)) & ROTM2;
    nxt.sa = mkrs(p.wd + (size_t)w * 448 * 1024 + n0 * 2, 0x7fffffffu); nxt.sb = nxt.sa;
    nxt.va = (l >> 3) * 1024 + swz; nxt.vb = nxt.va + 8192 - 1024; nxt.step = 16384; nxt.step2 = 16384; nxt.so = rfl((7 * rot2 - 1) * 16384); nxt.wrap = 28 * 16384;
    ra[0] = (nb & 1) ? raf[2] : raf[0]; ra[1] = (nb & 1) ? raf[3] : raf[1];
    g1_pass<2, 2>(acc, xf, ra, cur, nxt, wdst, xv, xrs, rot, nopub, p.ws);
    cur = nxt;
    ffn_epi(IC<2>{}, (20 + (nb >> 1)) * 4 + (nb & 1) * 2);
    drain_bar();
    st_flag(rs, sel(tid < 2, F2_OFF + mb * 256 + l * 128 + nb * 8, OOR), tag);
    FSTAMP(6);
    __builtin_amdgcn_s_setprio(0);
    wait_flags<4>(p.ws + sel(l & 16, F0_OFF + (256 + tt0 * 4 + (l & 7)) * 8, F2_OFF + mb * 256 + (nb & 1) * 128 + (l & 15) * 8), tag);
    __builtin_amdgcn_s_setprio(GPRIO);
    FSTAMP(7);
    const int wt = w & 1, wk = w >> 1;
    const float gate = __hip_atomic_load((const GAS float*)(p.ws + GT_OFF + ((tt0 + wt) * 32 + (l & 31)) * 4), RLX_AGENT);
    const rsrc_t ha = mkrs(p.ws + HF_OFF + ((size_t)tt0 * 112 + w * 28) * 1024, 28 * 1024), hb = mkrs(p.ws + HF_OFF + ((size_t)(tt0 + 1) * 112 + w * 28) * 1024, 28 * 1024);
    unsigned hso = rfl(rot2 * 7168);
    u32x4 hf[8][2];
    sfor<0, 7>([&](auto J) { hld2(hf[J.value][0], hf[J.value][1], lv, ha, hb, hso); });
    unsigned r2[2];
    for (int t = 0; t < 2; ++t) r2[t] = wdst + (4 * h + q) * 128 + pp * 8 + (((2 * t + g) ^ qh2) * 32);
    u32x4 wf0 = trfrag(r2[0], r2[0] + 1024), wf1 = trfrag(r2[1], r2[1] + 1024);
    f32x16 accB[4] = {};
    const rsrc_t wors = mkrs(p.wo + (size_t)w * 128 * 1024 + n0 * 2, 0x7fffffffu);
    for (int it = 0; it < 4; ++it) {
        if (hso == 28 * 1024) hso = 0;
        if (it == 3) hso = 0x70000000u;
        sfor<0, 7>([&](auto J) {
            constexpr int j = J.value, on = ((j + 1) % 7) * 8192;
            waitv2<(12 + 2 * j < 18 ? 12 + 2 * j : 18)>(hf[j][0], hf[j][1]);
            accB[0] = MFMA(BF8(wf0), BF8(hf[j][0]), accB[0]); SBAR();
            if constexpr (j == 1) { if (it == 3) { cur.sa = wors; cur.so = 0u - 16384; } }
            cur.sb = cur.sa;
            wdma<((j + 6) % 7) * 8192>(cur, wdst); SBAR();
            accB[1] = MFMA(BF8(wf0), BF8(hf[j][1]), accB[1]); SBAR();
            wf0 = trfrag(r2[0] + on, r2[0] + on + 1024); SBAR();
            accB[2] = MFMA(BF8(wf1), BF8(hf[j][0]), accB[2]); SBAR();
            accB[3] = MFMA(BF8(wf1), BF8(hf[j][1]), accB[3]); SBAR();
            wf1 = trfrag(r2[1] + on, r2[1] + on + 1024);
            hld2(hf[j][0], hf[j][1], lv, ha, hb, hso); SBAR();
        });
    }
    FSTAMP(8);
    wdma<6 * 8192>(cur, wdst); wdma<7 * 8192>(cur, wdst);
    sfor<0, 7>([&](auto J) { waitv2<0>(hf[J.value][0], hf[J.value][1]); });
    drain_bar();
    u32x4 wfo[16][2];
    { const unsigned d = wk * 4096 - w * 2048;
      sfor<0, 16>([&](auto M) { constexpr int on = (M.value & 7) * 8192 + (M.value >> 3) * 2048;
          wfo[M.value][0] = trfrag(r2[0] + d + on, r2[0] + d + on + 1024); wfo[M.value][1] = trfrag(r2[1] + d + on, r2[1] + d + on + 1024); }); }
    lds_bar();
    sfor<0, 4>([&](auto T) { constexpr int t = T.value;
        for (int r = 0; r < 4; ++r) {
            f32x4 v = {accB[t][4 * r], accB[t][4 * r + 1], accB[t][4 * r + 2], accB[t][4 * r + 3]};
            ldsw128(ring + ((t * 4 + w) * 4 + r) * 1024 + lv, __builtin_bit_cast(u32x4, v)); } });
    lds_bar();
    f32x16 own;
    for (int r = 0; r < 4; ++r) {
        f32x4 v = __builtin_bit_cast(f32x4, ldsr128(ring + ((w * 4) * 4 + r) * 1024 + lv));
        for (int o = 1; o < 4; ++o) v += __builtin_bit_cast(f32x4, ldsr128(ring + ((w * 4 + o) * 4 + r) * 1024 + lv));
        v *= 1.0f - gate;
        own[4 * r] = v[0]; own[4 * r + 1] = v[1]; own[4 * r + 2] = v[2]; own[4 * r + 3] = v[3]; }
    PIN(own);
    const rsrc_t oa = mkrs(p.ws + OF_OFF + ((size_t)(tt0 + wt) * 32 + wk * 16) * 1024, 8 * 1024), ob = mkrs(p.ws + OF_OFF + ((size_t)(tt0 + wt) * 32 + wk * 16 + 8) * 1024, 8 * 1024);
    char* orow = p.out + ((size_t)(tt0 + wt) * 32 + (l & 31)) * 1024 + (n0 + wk * 32) * 2 + h * 16;
    __builtin_amdgcn_s_setprio(0);
    wait_flags<1>(p.ws + F3_OFF + (tt0 >> 1) * 256 + (wk * 16 + (l & 15)) * 8, tag);
    __builtin_amdgcn_s_setprio(3);
    FSTAMP(9);
    { unsigned oso = 0;
      sfor<0, 8>([&](auto J) { hld2(hf[J.value][0], hf[J.value][1], lv, oa, ob, oso); }); }
    f32x16 accA[2];
    sfor<0, 8>([&](auto MM) { constexpr int m = MM.value;
        waitv2<14 - 2 * m>(hf[m][0], hf[m][1]);
        sfor<0, 2>([&](auto T) { constexpr int t = T.value;
            if constexpr (m == 0) accA[t] = MFMA(BF8(wfo[0][t]), BF8(hf[0][0]), f32x16{});
            else accA[t] = MFMA(BF8(wfo[m][t]), BF8(hf[m][0]), accA[t]);
            accA[t] = MFMA(BF8(wfo[m + 8][t]), BF8(hf[m][1]), accA[t]); });
        SBAR();
    });
    { const f32x16 snd = wk ? accA[0] : accA[1];
      for (int r = 0; r < 4; ++r) { f32x4 v = {snd[4 * r], snd[4 * r + 1], snd[4 * r + 2], snd[4 * r + 3]};
          ldsw128(ring + (w * 16 + r) * 1024 + lv, __builtin_bit_cast(u32x4, v)); } }
    { const f32x16 kp = wk ? accA[1] : accA[0];
      for (int r = 0; r < 16; ++r) own[r] += gate * kp[r]; }
    lds_bar();
    for (int r = 0; r < 4; ++r) {
        const f32x4 v = __builtin_bit_cast(f32x4, ldsr128(ring + ((w ^ 2) * 16 + r) * 1024 + lv));
        own[4 * r] += gate * v[0]; own[4 * r + 1] += gate * v[1]; own[4 * r + 2] += gate * v[2]; own[4 * r + 3] += gate * v[3]; }
    for (int i = 0; i < 4; i += 2) {
        unsigned a0 = cvtpk(own[4 * i], own[4 * i + 1]), a1 = cvtpk(own[4 * i + 2], own[4 * i + 3]);
        unsigned b0 = cvtpk(own[4 * i + 4], own[4 * i + 5]), b1 = cvtpk(own[4 * i + 6], own[4 * i + 7]);
        auto r0 = __builtin_amdgcn_permlane32_swap(a0, b0, false, false);
        auto r1 = __builtin_amdgcn_permlane32_swap(a1, b1, false, false);
        u32x4 v = {r0[0], r1[0], r0[1], r1[1]};
        *(u32x4*)(orow + i * 16) = v;
    }
    FSTAMP(10);
}

DI f32x16 smf(u32x4 a, f32x8 b, f32x16 c, int idx) {
    return __builtin_amdgcn_smfmac_f32_32x32x32_bf16(BF8(a), __builtin_bit_cast(bf16x16, b), c, idx, 0, 0);
}
DI f32x8 lo8(const f32x16& s) { return __builtin_shufflevector(s, s, 0, 1, 2, 3, 4, 5, 6, 7); }
DI f32x8 hi8(const f32x16& s) { return __builtin_shufflevector(s, s, 8, 9, 10, 11, 12, 13, 14, 15); }
template <int B, int E> DI void exps(f32x16& s) {
#pragma unroll
    for (int i = B; i < E; ++i) { float t = __builtin_amdgcn_exp2f(s[i]); PIN(t); s[i] = t; }
}
DI float rowmax16(const f32x16& s) {
    float a = __builtin_fmaxf(s[0], s[1]);
#pragma unroll
    for (int i = 2; i < 16; ++i) a = __builtin_fmaxf(a, s[i]);
    return a;
}

DI void a_role(const Params& p, int ai, char* lds) {
    const int tid = threadIdx.x, l = tid & 63, w = __builtin_amdgcn_readfirstlane(tid >> 6);
    const int head = (ai & 7) * 4 + ((ai >> 3) & 3), qb = ai >> 5, qh = w & 1, kh = w >> 1;
    const unsigned base = (unsigned)(uintptr_t)lds;
    const rsrc_t rs = mkrs(p.ws, WS_USED);
    const rsrc_t rkv = mkrs(p.ws + KV_OFF + (size_t)(head * 64 + kh * 32) * KVT, 32 * KVT);
    const unsigned lv = l * 16;
    FSTAMP(0);
    const u64 epoch = rfl64(ld_flag(p.ws + FL_OFF));
    const u64 tag = mix64(epoch);
    {
      const int tok = ai * 8 + w * 2 + (l >> 5);
      const u32x4* xr = (const u32x4*)(p.x + (size_t)tok * 1024 + (l & 31) * 32); const u32x4* gr = (const u32x4*)(p.gw + (l & 31) * 32);
      const u32x4 x0 = xr[0], x1 = xr[1], g0 = gr[0], g1 = gr[1];
      float z = 0.f;
      for (int i = 0; i < 4; ++i) {
          z += __uint_as_float(x0[i] << 16) * __uint_as_float(g0[i] << 16) + __uint_as_float(x0[i] & 0xffff0000u) * __uint_as_float(g0[i] & 0xffff0000u);
          z += __uint_as_float(x1[i] << 16) * __uint_as_float(g1[i] << 16) + __uint_as_float(x1[i] & 0xffff0000u) * __uint_as_float(g1[i] & 0xffff0000u); }
      for (int d = 1; d < 32; d <<= 1) z += __shfl_xor(z, d);
      __builtin_amdgcn_raw_buffer_store_b32(__float_as_uint(1.0f / (1.0f + __expf(-z))), rs, sel((l & 31) == 0, GT_OFF + tok * 4, OOR), 0, 16); }
    drain_bar();
    st_flag(rs, sel(tid == 0, F0_OFF + (256 + ai) * 8, OOR), tag);
    auto bump = [&]() {
        if (ai == 0 && w == 0) {
            const u32x2 t2 = __builtin_bit_cast(u32x2, tag);
#pragma nounroll
            for (unsigned spins = 0; spins < SPIN_CAP; ++spins) {
                unsigned d = 0;
                for (int i = 0; i < 4; ++i) { const u32x4 f = ld16_dev(rs, lv, F0_OFF + i * 1024); d |= (f[0] ^ t2[0]) | (f[1] ^ t2[1]) | (f[2] ^ t2[0]) | (f[3] ^ t2[1]); }
                if (__all(d == 0)) break;
                asm volatile("s_sleep 8" ::: "memory");
            }
            st_flag(rs, sel(l == 0, FL_OFF, OOR), epoch + 1);
        } };
    const int qt = qb * 8 + qh * 4;
    const int g = (l >> 4) & 1;
    const unsigned oo = ((l & 11) == 0) ? 2048u : OOR;
    const unsigned v0o = g ? oo : 1024u + ((l >> 5) * 16 + (l & 15)) * 16, v1o = g ? oo : v0o + 512;
    int idx = 0xDDDD; PIN(idx);
    f32x16 o[4] = {};
    for (int i = 0; i < 4; ++i) PIN(o[i]);
    __builtin_amdgcn_s_sleep(64);
    __builtin_amdgcn_s_setprio(APRIO);
    wait_flags<2>(p.ws + F1_OFF + head * 384 + sel(l & 16, (qb * 2 + qh) * 8, 128 + (l & 8) * 16 + (kh * 8 + (l & 7)) * 8), tag);
    FSTAMP(1);
    u32x4 qf[4];
    int nmax = 1;
    for (int attempt = 0; attempt < 2; ++attempt) {
        unsigned so = qb * 4 * KVT;
        u32x4 kA = ld16_dev(rkv, lv, so);
        for (int i = 0; i < 4; ++i) qf[i] = ld16_dev(rs, lv, QF_OFF + (head * 64 + qt + i) * 1024);
        SBAR();
        u32x4 v0A = ld16_dev(rkv, v0o, so), v1A = ld16_dev(rkv, v1o, so);
        SBAR();
        u32x4 kB = ld16_dev(rkv, lv + KVT, so);
        SBAR();
        u32x4 v0B = ld16_dev(rkv, v0o + KVT, so), v1B = {};
        SBAR();
        float mx[4];
        for (int i = 0; i < 4; ++i) mx[i] = rowmax16(MFMA(BF8(kA), BF8(qf[i]), f32x16{}));
        for (int t = 0; t < nmax - 1; ++t) {
            const u32x4 kf = ld16_dev(rkv, lv, t * KVT);
            for (int i = 0; i < 4; ++i) mx[i] = __builtin_fmaxf(mx[i], rowmax16(MFMA(BF8(kf), BF8(qf[i]), f32x16{})));
        }
        f32x16 c[4];
        for (int i = 0; i < 4; ++i) {
            auto rr = __builtin_amdgcn_permlane32_swap(__float_as_uint(mx[i]), __float_as_uint(mx[i]), false, false);
            mx[i] = __builtin_fmaxf(__uint_as_float(rr[0]), __uint_as_float(rr[1]));
            for (int r = 0; r < 16; ++r) c[i][r] = -mx[i];
            PIN(c[i]); }
        f32x16 sc = MFMA(BF8(kA), BF8(qf[0]), c[0]), sn;
        f32x8 ph = {};
        FSTAMP(2);
        auto tile = [&](u32x4& K, u32x4& V0, u32x4& V1, u32x4& Kn, u32x4& V1p, unsigned o1, unsigned o2, auto ODD) {
            constexpr unsigned a1 = ODD.value ? 0 : KVT, a2 = ODD.value ? KVT : 0;
            sfor<0, 4>([&](auto I) {
                constexpr int i = I.value, in = (i + 1) & 3, ip = (i + 3) & 3;
                SBAR();
                sn = MFMA(BF8(i < 3 ? K : Kn), BF8(qf[in]), c[in]); SBAR();
                if constexpr (i == 2) { K = ld16_dev(rkv, lv + a2, o2); SBAR(); }
                exps<0, 5>(sc); SBAR();
                o[ip] = smf(i == 0 ? V1p : V1, ph, o[ip], idx); SBAR();
                if constexpr (i == 0) { V1p = ld16_dev(rkv, v1o + a1, o1); SBAR(); }
                exps<5, 8>(sc); SBAR();
                exps<8, 11>(sc); SBAR();
                o[i] = smf(V0, lo8(sc), o[i], idx); SBAR();
                if constexpr (i == 3) { V0 = ld16_dev(rkv, v0o + a2, o2); SBAR(); }
                exps<11, 16>(sc); SBAR();
                ph = hi8(sc); sc = sn;
            });
        };
        for (int t = 0; t < 16; ++t) {
            unsigned sn2 = so + 2 * KVT; if (sn2 == 32 * KVT) sn2 = 0;
            tile(kA, v0A, v1A, kB, v1B, so, sn2, IC<0>{});
            tile(kB, v0B, v1B, kA, v1A, sn2, sn2, IC<1>{});
            so = sn2;
        }
        o[3] = smf(v1B, ph, o[3], idx);
        FSTAMP(3);
        sfor<0, 2>([&](auto U) { constexpr int u = U.value;
            const f32x16& s = kh ? o[u] : o[2 + u]; const float m = kh ? mx[u] : mx[2 + u];
            const unsigned a = base + (w * 2 + u) * 3072 + lv;
            f32x4 x0 = {s[0], s[1], s[2], s[3]}, x1 = {s[4], s[5], s[6], s[7]}, x2 = {s[8], m, 0.f, 0.f};
            ldsw128(a, __builtin_bit_cast(u32x4, x0)); ldsw128(a + 1024, __builtin_bit_cast(u32x4, x1)); ldsw128(a + 2048, __builtin_bit_cast(u32x4, x2)); });
        float big = 0.f;
        for (int i = 0; i < 4; ++i) { big += o[i][8] * 1.0e19f;
            for (int r = 0; r < 8; ++r) big += __builtin_fabsf(o[i][r]); }
        *(LAS int*)(uintptr_t)(base + 32768 + w * 4) = __any(!(big < 1.0e37f)) ? 1 : 0;
        lds_bar();
        const u32x4 bw = ldsr128(base + 32768);
        const int redo = attempt == 0 ? (int)(bw[0] | bw[1] | bw[2] | bw[3]) : 0;
        if (!__builtin_amdgcn_readfirstlane(redo)) {
            sfor<0, 2>([&](auto U) { constexpr int u = U.value;
                const f32x16& s = kh ? o[2 + u] : o[u]; const float m = kh ? mx[2 + u] : mx[u];
                const unsigned a = base + ((w ^ 2) * 2 + u) * 3072 + lv;
                const f32x4 x0 = __builtin_bit_cast(f32x4, ldsr128(a)), x1 = __builtin_bit_cast(f32x4, ldsr128(a + 1024)), x2 = __builtin_bit_cast(f32x4, ldsr128(a + 2048));
                const float mm = __builtin_fmaxf(m, x2[1]);
                float fa = __builtin_amdgcn_exp2f(m - mm), fb = __builtin_amdgcn_exp2f(x2[1] - mm);
                const float r = __builtin_amdgcn_rcpf(s[8] * fa + x2[0] * fb);
                fa *= r; fb *= r;
                u32x4 v = {cvtpk(s[0] * fa + x0[0] * fb, s[1] * fa + x0[1] * fb), cvtpk(s[2] * fa + x0[2] * fb, s[3] * fa + x0[3] * fb),
                           cvtpk(s[4] * fa + x1[0] * fb, s[5] * fa + x1[1] * fb), cvtpk(s[6] * fa + x1[2] * fb, s[7] * fa + x1[3] * fb)};
                st16_wt(rs, lv, OF_OFF + ((qt + kh * 2 + u) * 32 + head) * 1024, v); });
            break;
        }
        lds_bar();
        nmax = 33;
        for (int i = 0; i < 4; ++i) o[i] = f32x16{};
    }
    waitv<0>();
    st_flag(rs, sel(l == 0, F3_OFF + ((qt >> 1) + kh) * 256 + head * 8, OOR), tag);
    FSTAMP(4);
    bump();
}

__global__ __launch_bounds__(256, 2) void k_fused(Params p) {
    extern __shared__ __attribute__((aligned(16))) char lds[];
    if (blockIdx.x < 256) { g_role(p, blockIdx.x, lds); __builtin_amdgcn_endpgm(); }
    a_role(p, blockIdx.x - 256, lds);
}

extern "C" void kernel_launch(void* const* d_in, const int* in_sizes, int n_in, void* d_out, int out_size, void* d_ws, size_t ws_size,
                              hipStream_t stream) {
    Params p;
    p.x = (const char*)d_in[0]; p.wqkv = (const char*)d_in[1]; p.wo = (const char*)d_in[2];
    p.wg = (const char*)d_in[4]; p.wu = (const char*)d_in[5]; p.wd = (const char*)d_in[6]; p.gw = (const char*)d_in[7];
    p.out = (char*)d_out; p.ws = (char*)d_ws;
    (void)hipFuncSetAttribute((const void*)k_fused, hipFuncAttributeMaxDynamicSharedMemorySize, LDS_BYTES);
    k_fused<<<512, 256, LDS_BYTES, stream>>>(p);
}
```

```cpp
#include <hip/hip_runtime.h>
#include <hip/hip_bf16.h>
#include <stdint.h>
#include <type_traits>

typedef __bf16 bf16x8 __attribute__((ext_vector_type(8)));
typedef __bf16 bf16x16 __attribute__((ext_vector_type(16)));
typedef float f32x16 __attribute__((ext_vector_type(16)));
typedef float f32x8 __attribute__((ext_vector_type(8)));
typedef float f32x4 __attribute__((ext_vector_type(4)));
typedef unsigned u32x4 __attribute__((ext_vector_type(4)));
typedef unsigned u32x2 __attribute__((ext_vector_type(2)));
typedef unsigned long long u64;
typedef __amdgpu_buffer_rsrc_t rsrc_t;
#define LAS __attribute__((address_space(3)))
#define GAS __attribute__((address_space(1)))
#define DI __device__ __forceinline__

constexpr unsigned QF_OFF = 0;
constexpr unsigned KVT = 2176;
constexpr unsigned KV_OFF = 2u << 20;
constexpr unsigned HF_OFF = 7u << 20;
constexpr unsigned OF_OFF = 14u << 20;
constexpr unsigned FL_OFF = 16u << 20;
constexpr unsigned F0_OFF = FL_OFF + 4096;
constexpr unsigned GT_OFF = FL_OFF + 8192;
#define FLS 128
constexpr unsigned F1_OFF = FL_OFF + 32768;
constexpr unsigned F2_OFF = FL_OFF + 65536;
constexpr unsigned F2B = 32768;
constexpr unsigned F3_OFF = FL_OFF + 131072;
constexpr unsigned WS_USED = 17u << 20;
constexpr int LDS_BYTES = 81920;
constexpr int XS_OFF = 65536;
constexpr float QSCALE = 0.25f * 1.4426950408889634f;
constexpr unsigned SPIN_CAP = 1u << 18;
#define W1A 8
#define W1B 2
#define P0 4
#define P1 7
#define P2 10
constexpr int SPFG = 64, SPFN = 8;
#define APRIO 2

struct Params {
    const char* x; const char* wqkv; const char* wo; const char* wg; const char* wu; const char* wd; const char* gw;
    char* out; char* ws;
};

template <int I> using IC = std::integral_constant<int, I>;
template <int B, int E, class F> DI void sfor(F&& f) { if constexpr (B < E) { f(IC<B>{}); sfor<B + 1, E>(f); } }

#define FSTAMP(i)
#define SLOTSTAMP(i)
#define SLOTDECL
#define SLOTOUT(m)
#define MFMA(a, b, c) __builtin_amdgcn_mfma_f32_32x32x16_bf16((a), (b), (c), 0, 0, 0)
#define SBAR() __builtin_amdgcn_sched_barrier(0)
#define MBAR() asm volatile("" ::: "memory")
#define PIN(x) asm volatile("" : "+v"(x))
#define PINS(x) asm volatile("" : "+s"(x))
#define RLX_AGENT __ATOMIC_RELAXED, __HIP_MEMORY_SCOPE_AGENT
#define BF8(x) __builtin_bit_cast(bf16x8, (x))

DI unsigned rfl(unsigned v) { return (unsigned)__builtin_amdgcn_readfirstlane((int)v); }
DI u64 rfl64(u64 v) { return ((u64)(unsigned)__builtin_amdgcn_readfirstlane((int)(v >> 32)) << 32) | (unsigned)__builtin_amdgcn_readfirstlane((int)v); }
DI rsrc_t mkrs(const char* p, unsigned bytes) { return __builtin_amdgcn_make_buffer_rsrc((void*)rfl64((u64)p), 0, (int)bytes, 0x00020000); }

struct WStream { rsrc_t sa, sb; unsigned va, vb, so, step, step2, wrap; };
template <int DST> DI void wdma(WStream& s, unsigned wdst) {
    asm volatile("s_add_u32 m0, %[wd], %[imm]\n\ts_add_u32 %[so], %[so], %[st]\n\t"
                 "buffer_load_dwordx4 %[va], %[sa], %[so] offen lds\n\tbuffer_load_dwordx4 %[vb], %[sb], %[so] offen offset:1024 lds"
                 : [so] "+s"(s.so) : [wd] "s"(wdst), [imm] "n"(DST), [st] "s"(s.step), [va] "v"(s.va), [vb] "v"(s.vb), [sa] "s"(s.sa), [sb] "s"(s.sb)
                 : "scc", "memory");
}
#define ROTM 0
template <int DST, int PIECE> DI void wdma1(WStream& s, unsigned wdst) {
    if constexpr (PIECE == 0 && ROTM != 0 && (DST & 8192) == 0)
        asm volatile("s_add_u32 m0, %[wd], %[imm]\n\ts_add_u32 %[so], %[so], %[st]\n\ts_cmp_lg_u32 %[so], %[wr]\n\ts_cselect_b32 %[so], %[so], 0\n\t"
                     "buffer_load_dwordx4 %[va], %[sa], %[so] offen lds"
                     : [so] "+s"(s.so) : [wd] "s"(wdst), [imm] "n"(DST), [st] "s"(s.step2), [wr] "s"(s.wrap), [va] "v"(s.va), [sa] "s"(s.sa) : "scc", "memory");
    else if constexpr (PIECE == 0)
        asm volatile("s_add_u32 m0, %[wd], %[imm]\n\ts_add_u32 %[so], %[so], %[st]\n\tbuffer_load_dwordx4 %[va], %[sa], %[so] offen lds"
                     : [so] "+s"(s.so) : [wd] "s"(wdst), [imm] "n"(DST), [st] "s"((DST & 8192) ? s.step : s.step2), [va] "v"(s.va), [sa] "s"(s.sa) : "scc", "memory");
    else
        asm volatile("buffer_load_dwordx4 %[vb], %[sb], %[so] offen offset:1024 lds" :: [so] "s"(s.so), [vb] "v"(s.vb), [sb] "s"(s.sb) : "memory");
}
template <int OFF> DI u32x4 xld16(unsigned voff, rsrc_t rs, unsigned so) {
    u32x4 r; asm volatile("buffer_load_dwordx4 %0, %1, %2, %3 offen offset:%4" : "=v"(r) : "v"(voff), "s"(rs), "s"(so), "n"(OFF) : "memory"); return r;
}
template <int X, int D> DI void qsel2(u32x4& r, const u32x4& a, const u32x4& b, u64 keep) {
    unsigned r0, r1; const unsigned a0 = a[D], a1 = a[D + 1], b0 = b[D], b1 = b[D + 1];
    register u64 kv asm("vcc") = keep;
    if constexpr (X == 1)
        asm volatile("v_cndmask_b32_dpp %0, %2, %4, vcc quad_perm:[1,0,3,2] row_mask:0xf bank_mask:0xf\n\tv_cndmask_b32_dpp %1, %3, %5, vcc quad_perm:[1,0,3,2] row_mask:0xf bank_mask:0xf"
                     : "=&v"(r0), "=v"(r1) : "v"(a0), "v"(a1), "v"(b0), "v"(b1), "s"(kv));
    else
        asm volatile("v_cndmask_b32_dpp %0, %2, %4, vcc quad_perm:[2,3,0,1] row_mask:0xf bank_mask:0xf\n\tv_cndmask_b32_dpp %1, %3, %5, vcc quad_perm:[2,3,0,1] row_mask:0xf bank_mask:0xf"
                     : "=&v"(r0), "=v"(r1) : "v"(a0), "v"(a1), "v"(b0), "v"(b1), "s"(kv));
    r[D] = r0; r[D + 1] = r1;
}
DI void hld2(u32x4& a, u32x4& b, unsigned voff, rsrc_t sa, rsrc_t sb, unsigned& so) {
    asm volatile("buffer_load_dwordx4 %0, %3, %4, %2 offen sc1\n\tbuffer_load_dwordx4 %1, %3, %5, %2 offen sc1"
                 : "=&v"(a), "=&v"(b) : "s"(so), "v"(voff), "s"(sa), "s"(sb) : "memory");
    so += 1024;
}
DI void hld2o(u32x4& a, u32x4& b, unsigned voff, rsrc_t sa, unsigned& so) {
    asm volatile("buffer_load_dwordx4 %0, %3, %4, %2 offen sc1\n\tbuffer_load_dwordx4 %1, %3, %4, %2 offen offset:1024 sc1"
                 : "=&v"(a), "=&v"(b) : "s"(so), "v"(voff), "s"(sa) : "memory");
    so += 2048;
}
template <int N> DI void waitv() { asm volatile("s_waitcnt vmcnt(%0)" ::"n"(N) : "memory"); }
template <int N> DI void waitv2(u32x4& a, u32x4& b) { asm volatile("s_waitcnt vmcnt(%2)" : "+v"(a), "+v"(b) : "n"(N) : "memory"); }
template <int N> DI void waitv_bar() { asm volatile("s_waitcnt vmcnt(%0)\n\ts_barrier" ::"n"(N) : "memory"); }
DI void lds_bar() { asm volatile("s_waitcnt lgkmcnt(0)\n\ts_barrier" ::: "memory"); }

DI unsigned cvtpk(float lo, float hi) {
    typedef __bf16 b2 __attribute__((ext_vector_type(2)));
    b2 r; r[0] = (__bf16)lo; r[1] = (__bf16)hi; return __builtin_bit_cast(unsigned, r);
}
DI u32x2 trread(unsigned addr) {
    typedef short v4i16_t __attribute__((ext_vector_type(4)));
    return __builtin_bit_cast(u32x2, __builtin_amdgcn_ds_read_tr16_b64_v4i16((LAS v4i16_t*)(uintptr_t)addr));
}
DI u32x4 trfrag(unsigned a0, unsigned a1) { u32x2 lo = trread(a0), hi = trread(a1); u32x4 r = {lo[0], lo[1], hi[0], hi[1]}; return r; }
DI u32x4 ldsr128(unsigned addr) { return *(const LAS u32x4*)(uintptr_t)addr; }
DI void ldsw128(unsigned addr, u32x4 v) { *(LAS u32x4*)(uintptr_t)addr = v; }
DI void st16_wt(rsrc_t rs, unsigned voff, unsigned soff, u32x4 v) {
    __builtin_amdgcn_raw_buffer_store_b128(v, rs, voff, soff, 16);
    asm volatile("s_nop 1" :: "v"(v) : "memory");
}
DI u32x4 ld16_dev(rsrc_t rs, unsigned voff, unsigned soff) { return __builtin_amdgcn_raw_buffer_load_b128(rs, voff, soff, 16); }

DI u64 mix64(u64 z) { z += 0x9E3779B97F4A7C15ull; z = (z ^ (z >> 30)) * 0xBF58476D1CE4E5B9ull; z = (z ^ (z >> 27)) * 0x94D049BB133111EBull; return z ^ (z >> 31); }
DI u64 ld_flag(const char* a) { return __hip_atomic_load((const GAS u64*)a, RLX_AGENT); }
constexpr unsigned OOR = 0x7ff00000u;
DI unsigned sel(bool c, unsigned a, unsigned b) { const unsigned m = 0u - (unsigned)c; return (a & m) | (b & ~m); }
DI void st_flag(rsrc_t rs, unsigned voff, u64 v) { __builtin_amdgcn_raw_buffer_store_b64(__builtin_bit_cast(u32x2, v), rs, voff, 0, 16); }
DI u64 poll_issue(rsrc_t rs, unsigned voff) {
    u64 r; asm volatile("buffer_load_dwordx2 %0, %1, %2, 0 offen sc1" : "=v"(r) : "v"(voff), "s"(rs) : "memory"); return r;
}
DI unsigned tag32(u64 epoch) { return (unsigned)epoch * 0x9E3779B1u + 0x7F4A7C15u; }
DI float get_gate(rsrc_t rs, unsigned voff, u64 gr, unsigned t32) {
#pragma nounroll
    for (unsigned spins = 0; spins < SPIN_CAP && __builtin_expect(!__all((unsigned)(gr >> 32) == t32), 0); ++spins)
        asm volatile("s_sleep 2\n\tbuffer_load_dwordx2 %0, %1, %2, 0 offen sc1\n\ts_waitcnt vmcnt(0)" : "=v"(gr) : "v"(voff), "s"(rs) : "memory");
    return __uint_as_float((unsigned)gr);
}
template <int SLEEP> DI void wait_flags(const char* a, u64 tag) {
#pragma nounroll
    for (unsigned spins = 0; spins < SPIN_CAP; ++spins) {
        if (__all(ld_flag(a) == tag)) break;
        __builtin_amdgcn_s_sleep(SLEEP);
    }
    asm volatile("" ::: "memory");
}

template <int S0, int S1> DI void wait_flags2(rsrc_t rs, unsigned voff, u64 tag) {
    const u64 all = __ballot(voff != OOR);
#pragma nounroll
    for (unsigned spins = 0; spins < SPIN_CAP; ++spins) {
        const u64 f = __builtin_bit_cast(u64, __builtin_amdgcn_raw_buffer_load_b64(rs, voff, 0, 16));
        voff = f == tag ? OOR : voff;
        const u64 m = __ballot(voff != OOR);
        if (m == 0) break;
        if (m == all) __builtin_amdgcn_s_sleep(S0); else __builtin_amdgcn_s_sleep(S1);
    }
    asm volatile("" ::: "memory");
}

#define NPRO 2
#define XSTEP 2
#define PUBS 3
#define PUBS2 3
constexpr int XW0 = 5;
constexpr int AHEAD = 6;
struct Op { int x, a, b, gap; };
struct Plan { Op op[96]; int n; };
constexpr int bar_gap(int s, int NT) { return s <= 0 ? -1 : (2 * s - 1) * NT; }
constexpr Plan mk_plan(int mode, int NT) {
    Plan P{};
    auto put = [&](int x, int a, int b, int gap) { P.op[P.n] = Op{x, a, b, gap}; ++P.n; };
    int ws = 0, wp = 0, xc = mode == 0 ? 0 : 8, xj = 0, g = 0;
    const int wend = 16 + AHEAD;
    if (mode == 0) for (int i = 0; i < NPRO; ++i) {
        for (int j = 0; j < 4; ++j) put(1, i, j, -1);
        for (int j = 0; j < 4; ++j) put(0, 2 * i + (j >> 1), j & 1, -1);
        xc = i + 1; ws = 2 * i + 2; }
    if (mode >= 1) { for (int j = 0; j < 2 * AHEAD; ++j) put(0, j >> 1, j & 1, -2); for (int j = 0; j < 4; ++j) put(2, 0, j, -2); ws = AHEAD; }
    while (g < 32 * NT && (ws < wend || xc < 8)) {
        const int e = (mode == 0 && ws < 8) ? -1 : bar_gap(ws - AHEAD, NT);
        const bool wok = ws < wend && g >= e + (wp ? NT : 0) + 1, behind = ws < wend && g > bar_gap(ws + 1 - AHEAD, NT);
        if (xc < 8 && (2 * xc <= ws || !wok)) { put(1, xc, xj, g); if (++xj == 4) { xj = 0; ++xc; } g += XSTEP; }
        else if (wok) { put(0, ws, wp, g); if (++wp == 2) { wp = 0; ++ws; } g += (mode == 0 && xc < 8) ? XSTEP : (behind ? 1 : 1); }
        else ++g;
    }
    return P;
}
constexpr int plan_wait(const Plan& P, int gap, int x, int a, int b) {
    int issued = 0, last = -1;
    for (int i = 0; i < P.n; ++i) { if (P.op[i].gap < gap || P.op[i].gap < 0) ++issued; if (P.op[i].x == x && P.op[i].a == a && P.op[i].b == b) last = i; }
    return last < 0 ? -1 : issued - last - 1;
}
constexpr int plan_first(const Plan& P, int gap) { int i = 0; while (i < P.n && P.op[i].gap < gap) ++i; return i; }
template <int MODE, int NT> struct PlanOf { static constexpr Plan P = mk_plan(MODE, NT); };

template <int MODE, int NT, class PUB>
DI void g1_pass(f32x16 (&acc)[4], u32x4 (&xf)[32], const unsigned (&ra_)[4], WStream& cur, WStream& nxt, unsigned wdst,
                unsigned xv, rsrc_t xrs, unsigned rot, PUB&& pub, char* wsdbg) {
    SLOTDECL;
    using PL = PlanOf<MODE, NT>;
    constexpr bool FIRST = MODE == 0;
    static_assert(plan_wait(PL::P, 999, 0, 15 + AHEAD, 1) == 0, "plan incomplete");
    u32x4 wf[2][4];
    unsigned ra[4];
    auto issue = [&](auto GG) {
        sfor<plan_first(PL::P, GG.value), plan_first(PL::P, GG.value + 1)>([&](auto I) {
            constexpr Op o = PL::P.op[I.value];
            if constexpr (o.x) xf[4 * o.a + o.b] = xld16<((o.b >> 1) + (o.b & 1) * 2) * 1024>(xv, xrs, ((o.a + rot) & 7) * 128);
            else if constexpr (o.a < 16) wdma1<(o.a & 7) * 8192, o.b>(cur, wdst);
            else wdma1<(o.a & 7) * 8192, o.b>(nxt, wdst); });
    };
    if constexpr (MODE == 0) issue(IC<-1>{});
    sfor<0, NT>([&](auto T) { ra[T.value] = ra_[T.value]; PIN(ra[T.value]); });
    u32x4 tq[4];
    auto xtr = [&](auto CC, auto MM, auto GG) {
        constexpr int c = CC.value, g = MM.value >> 1, d = (MM.value & 1) * 2;
        if constexpr (MM.value == 0 || MM.value == 4) {
            constexpr int nw = plan_wait(PL::P, GG.value, 1, c, g + 1);
            static_assert(nw >= 0 && nw < 64, "x chunk issued too late");
            waitv2<nw>(xf[4 * c + g], xf[4 * c + g + 1]);
        }
        if constexpr (g < 4) qsel2<2, d>(tq[g], xf[4 * c + (g ^ 1)], xf[4 * c + g], (g & 1) ? 0xCCCCCCCCCCCCCCCCull : 0x3333333333333333ull);
        else { constexpr int i = g - 4, u = (i >> 1) + (i & 1) * 2;
               qsel2<1, d>(xf[4 * c + i], tq[u ^ 2], tq[u], (i & 1) ? 0xAAAAAAAAAAAAAAAAull : 0x5555555555555555ull); }
    };
    auto rd = [&](auto NN, auto T) {
        constexpr int n = NN.value, off = ((n >> 1) & 7) * 8192 + (n & 1) * 4096;
        wf[n & 1][T.value] = trfrag(ra[T.value] + off, ra[T.value] + off + 512);
    };
    auto bar = [&](auto SS) {
        constexpr int s = SS.value, nw = plan_wait(PL::P, bar_gap(s, NT), 0, s, 1);
        static_assert(nw >= 0 && nw < 64, "slot issued too late");
        if constexpr (MODE >= 1 && s == (MODE == 1 ? PUBS : PUBS2)) { constexpr int ns = plan_wait(PL::P, bar_gap(s, NT), 2, 0, 3); waitv_bar<(ns < nw ? ns : nw)>(); pub(IC<0>{}); }
        else waitv_bar<nw>();
        if constexpr (s == 15) pub(IC<1>{});
        SLOTSTAMP(MODE * 16 + s);
    };
    if constexpr (FIRST) sfor<0, 16 - XW0>([&](auto M) { xtr(IC<0>{}, M, IC<-1>{}); });
    bar(IC<0>{});
    sfor<0, NT>([&](auto T) { rd(IC<0>{}, T); });
    if constexpr (MODE >= 1) __builtin_amdgcn_s_setprio(0);
    SBAR();
    sfor<0, 32>([&](auto NN) {
        constexpr int n = NN.value;
        sfor<0, NT>([&](auto T) {
            constexpr int t = T.value;
            if constexpr (n == 0) acc[t] = MFMA(BF8(wf[0][t]), BF8(xf[0]), f32x16{});
            else acc[t] = MFMA(BF8(wf[n & 1][t]), BF8(xf[n]), acc[t]);
            SBAR();
            if constexpr (n + 1 < 32) {
                if constexpr ((n & 1) == 1 && t == 0) bar(IC<(n + 1) / 2 + t>{});
                rd(IC<n + 1>{}, T);
            }
            constexpr int gx = n * 4 + t + 16 - XW0;
            if constexpr (FIRST && gx < 128) xtr(IC<gx / 16>{}, IC<gx % 16>{}, IC<n * NT + t>{});
            issue(IC<n * NT + t>{});
            SBAR();
        });
    });
    if constexpr (MODE <= 1) __builtin_amdgcn_s_setprio(3);
    SLOTOUT(MODE);
}

DI void g_role(const Params& p, int gi, char* lds) {
    const int tid = threadIdx.x, l = tid & 63, w = __builtin_amdgcn_readfirstlane(tid >> 6);
    const int h = l >> 5, g = (l >> 4) & 1, q = (l >> 2) & 3, pp = l & 3;
    const int mb2 = ((gi & 7) >> 1) * 4 + ((gi >> 3) & 3), nb2 = (gi >> 5) * 2 + (gi & 1);
    const int mb = (gi & 1) * 8 + ((gi >> 3) & 7), nb = (gi >> 6) * 4 + ((gi >> 1) & 3), h3 = (gi & 6) * 2 + (gi >> 6);
    const unsigned ring = (unsigned)(uintptr_t)lds, wdst = ring + w * 2048;
    const int tt = mb * 4 + w;
    FSTAMP(0);
    const bool hasq = nb < 12;
    const unsigned swz = (((l >> 1) & 3) ^ (2 * ((l >> 4) & 1))) * 32 + (l & 1) * 16;
    const unsigned rot = ((gi >> 5) + 2 * (gi >> 3)) & ROTM;
    const unsigned wrow = 8 * (w >> 1) + 32 * (w & 1) + (l >> 3);
    auto ffn_stream = [&](int gq) { WStream s;
        s.sa = mkrs(p.wg + gq * 128, 0x7fffffffu); s.sb = mkrs(p.wu + gq * 128 - 1024, 0x7fffffffu);
        s.va = wrow * 3584 + swz; s.vb = s.va ^ 32; s.step = 16 * 3584; s.step2 = 48 * 3584; s.so = rfl((4 * rot - 3) * s.step); s.wrap = 512 * 3584; return s; };
    WStream cur, nxt;
    if (hasq) { cur.sa = mkrs(p.wqkv + nb * 256, 0x7fffffffu); cur.sb = mkrs(p.wqkv + nb * 256 + 128 - 1024, 0x7fffffffu);
        cur.va = wrow * 3072 + swz; cur.vb = cur.va ^ 32; cur.step = 16 * 3072; cur.step2 = 48 * 3072; cur.so = rfl((4 * rot - 3) * cur.step); cur.wrap = 512 * 3072; }
    else cur = ffn_stream(4 + nb);
    u32x4 xf[32];
    const rsrc_t xrs = mkrs(p.x + (size_t)tt * 32 * 1024, 32 * 1024); const unsigned xv = ((l >> 2) & 7) * 4096 + h * 64 + pp * 16;
    u64 epv;
    asm volatile("global_load_dwordx2 %0, %1, %2 sc1" : "=v"(epv) : "v"(0u), "s"(p.ws + FL_OFF) : "memory");
    nxt = ffn_stream(nb);
    const unsigned xs = ring + XS_OFF + w * 4096;
    const unsigned rbase = ring + h * 2048 + q * 128 + pp * 8;
    const int qh2 = (q >> 1) * 2;
    const rsrc_t rs = mkrs(p.ws, WS_USED);
    const unsigned lv = l * 16;
    f32x16 acc[4];
    unsigned ra[4], raf[4];
    for (int t = 0; t < 4; ++t) {
        raf[t] = rbase + g * 1024 + ((t ^ qh2 ^ g) * 32);
        ra[t] = hasq ? rbase + (t >> 1) * 1024 + (((2 * (t & 1) + g) ^ qh2 ^ (t >> 1)) * 32) : raf[t];
    }
    FSTAMP(1);
    auto pub0 = [](auto) {};
    g1_pass<0, 4>(acc, xf, ra, cur, nxt, wdst, xv, xrs, rot, pub0, p.ws);
    FSTAMP(2);
    PIN(epv);
    const u64 epoch = rfl64(epv), tag = mix64(epoch);
    auto ffn_epi = [&](auto NT, int ks0) {
        sfor<0, NT.value>([&](auto T) { constexpr int t = T.value;
            u32x4 v;
            for (int i = 0; i < 4; ++i) { float c0 = acc[t][2 * i] * acc[t][8 + 2 * i], c1 = acc[t][2 * i + 1] * acc[t][9 + 2 * i]; PIN(c0); v[i] = cvtpk(c0, c1); }
            st16_wt(rs, lv, HF_OFF + (tt * 112 + ks0 + t) * 1024, v); });
    };
    if (nb < 8) {
        const float sc = nb < 4 ? QSCALE : 1.0f;
        const unsigned hs = nb < 4 ? 64 * 1024 : 64 * KVT;
        const unsigned base = nb < 4 ? QF_OFF + nb * 8 * hs + tt * 1024 : KV_OFF + (nb - 4) * 8 * hs + tt * KVT;
        sfor<0, 8>([&](auto HH) { constexpr int t = HH.value >> 1, o = (HH.value & 1) * 8;
            u32x4 v = {cvtpk(acc[t][o] * sc, acc[t][o + 1] * sc), cvtpk(acc[t][o + 2] * sc, acc[t][o + 3] * sc), cvtpk(acc[t][o + 4] * sc, acc[t][o + 5] * sc), cvtpk(acc[t][o + 6] * sc, acc[t][o + 7] * sc)};
            st16_wt(rs, lv, base + HH.value * hs, v); });
    } else if (nb < 12) {
        const unsigned base = KV_OFF + (nb - 8) * 8 * 64 * KVT + tt * KVT;
        const u32x4 one = {0x3f803f80u, 0x3f803f80u, 0x3f803f80u, 0x3f803f80u};
        st16_wt(rs, (l >> 3) * 64 * KVT + 2048 + (l & 7) * 16, base, one);
        const unsigned wa = xs + h * 512 + (((l & 31) ^ (4 * h)) * 16);
        const unsigned ta = xs + g * 1024 + (pp & 1) * 512 + (((8 * h + q) ^ (4 * (pp & 1))) * 16) + 8 * (pp >> 1);
        const unsigned so = g * 64 * KVT + 1024 + (h * 16 + (l & 15)) * 16;
        sfor<0, 4>([&](auto T) { constexpr int t = T.value, lo = (t & 1) * 2048;
            sfor<0, 2>([&](auto HH) { constexpr int o = HH.value * 8;
                u32x4 v = {cvtpk(acc[t][o], acc[t][o + 1]), cvtpk(acc[t][o + 2], acc[t][o + 3]), cvtpk(acc[t][o + 4], acc[t][o + 5]), cvtpk(acc[t][o + 6], acc[t][o + 7])};
                ldsw128(wa + lo + HH.value * 1024, v); });
            const u32x4 f0 = trfrag(ta + lo, (ta ^ 64) + lo), f1 = trfrag(ta + lo + 256, (ta ^ 64) + lo + 256);
            st16_wt(rs, so, base + t * 2 * 64 * KVT, f0); st16_wt(rs, so + 512, base + t * 2 * 64 * KVT, f1); });
    } else ffn_epi(IC<4>{}, (4 + nb) * 4);
    FSTAMP(3);
    const unsigned f1o = sel(tid < (hasq ? 1 : 0), F1_OFF + (nb * 16 + mb) * FLS, sel(tid == 1, F0_OFF + gi * 8, OOR));
    auto pub1 = [&](auto K) { if constexpr (K.value == 0) { st_flag(rs, f1o, tag); FSTAMP(4); } };
    cur = ffn_stream(20 + (h3 >> 1));
    cur.va = sel(((swz >> 6) ^ h3) & 1, 0x80000000u, cur.va); cur.vb = cur.va ^ 32;
    g1_pass<1, 4>(acc, xf, raf, nxt, cur, wdst, xv, xrs, rot, pub1, p.ws);
    ffn_epi(IC<4>{}, nb * 4);
    FSTAMP(5);
    const int tt0 = mb2 * 4 + (nb2 & 1) * 2, n0 = (nb2 >> 1) * 64;
    nxt.sa = mkrs(p.wd + n0 * 2, 0x7fffffffu); nxt.sb = nxt.sa;
    nxt.va = (l >> 3) * 1024 + swz; nxt.vb = nxt.va + 8192 - 1024; nxt.step = 16384; nxt.step2 = 16384; nxt.so = rfl((20 * w - 1) * 16384); nxt.wrap = 0xffffffffu;
    ra[0] = (h3 & 1) ? raf[2] : raf[0]; ra[1] = (h3 & 1) ? raf[3] : raf[1];
    const unsigned f2o = sel(tid == 0, F2_OFF + (mb * 16 + nb) * FLS, OOR);
    unsigned f2p = F2_OFF + (mb2 * 16 + (l & 15)) * FLS;
    u64 fl;
    auto pub2 = [&](auto K) { if constexpr (K.value == 0) st_flag(rs, f2o, tag); else fl = poll_issue(rs, f2p); };
    g1_pass<2, 2>(acc, xf, ra, cur, nxt, wdst, xv, xrs, rot, pub2, p.ws);
    cur = nxt;
    ffn_epi(IC<2>{}, 80 + h3 * 2);
    FSTAMP(6);
    const int wt = w & 1, wk = w >> 1;
    const rsrc_t ha = mkrs(p.ws + HF_OFF + (size_t)(tt0 + wt) * 112 * 1024, 112 * 1024), hb = mkrs(p.ws + HF_OFF + (size_t)(tt0 + 1 - wt) * 112 * 1024, 112 * 1024);
    unsigned hso = rfl(w * 20480);
    u32x4 hf[8][2];
    auto first_loads = [&]() {
        sfor<0, 7>([&](auto J) { hld2(hf[J.value][0], hf[J.value][1], lv, ha, hb, hso); }); };
    asm volatile("s_waitcnt vmcnt(4)" : "+v"(fl) :: "memory");
    const bool early = __all(fl == tag);
    if (early) first_loads();
    asm volatile("s_waitcnt vmcnt(0) lgkmcnt(0)\n\ts_barrier" : "+v"(fl) :: "memory");
    st_flag(rs, f2o + F2B, tag);
    if (!early) {
        wait_flags<4>(p.ws + f2p, tag);
        first_loads();
        waitv<0>(); }
    FSTAMP(7);
    f2p += F2B;
    WStream wos = cur;
    wos.sa = mkrs(p.wo + (size_t)w * 128 * 1024 + n0 * 2, 0x7fffffffu); wos.sb = wos.sa; wos.so = 4 * 16384;
    wdma<7 * 8192>(wos, wdst); wdma<8 * 8192>(wos, wdst); wdma<9 * 8192>(wos, wdst);
    unsigned r2[2];
    for (int t = 0; t < 2; ++t) r2[t] = wdst + (4 * h + q) * 128 + pp * 8 + (((2 * (t ^ wk) + g) ^ qh2) * 32);
    u32x4 wf0 = trfrag(r2[0], r2[0] + 1024), wf1 = trfrag(r2[1], r2[1] + 1024);
    f32x16 accB[4];
    { u32x4 z = {}; PIN(z);
      for (int i = 0; i < 4; ++i) { accB[i] = MFMA(BF8(z), BF8(z), f32x16{}); PIN(accB[i]); PIN(z); } }
    for (int it = 0; it < 4; ++it) {
        if (it == 3) hso = OOR;
        if (it == 3) f2p = GT_OFF + (tt0 * 32 + (l ^ (wt * 32))) * 8;
        sfor<0, 7>([&](auto J) {
            constexpr int j = J.value, on = ((j + 1) % 7) * 8192;
            waitv2<18>(hf[j][0], hf[j][1]);
            PIN(wf0); accB[0] = MFMA(BF8(wf0), BF8(hf[j][0]), accB[0]); PIN(accB[0]); SBAR();
            if constexpr (j == 0) { if (it == 2) cur.so = (79 + 8 * w) * 16384; }
            if constexpr (j == 1) { if (it == 3) { cur.sa = wos.sa; cur.so = 0u - 16384; } }
            if constexpr (j == 6) { if (it == 3) cur.so = 0x80000000u; }
            cur.sb = cur.sa;
            wdma<((j + 6) % 7) * 8192>(cur, wdst); SBAR();
            PIN(wf0); accB[1] = MFMA(BF8(wf0), BF8(hf[j][1]), accB[1]); PIN(accB[1]); SBAR();
            wf0 = trfrag(r2[0] + on, r2[0] + on + 1024); SBAR();
            PIN(wf1); accB[2] = MFMA(BF8(wf1), BF8(hf[j][0]), accB[2]); PIN(accB[2]); SBAR();
            if constexpr (j == 3) { fl = poll_issue(rs, f2p); SBAR(); }
            PIN(wf1); accB[3] = MFMA(BF8(wf1), BF8(hf[j][1]), accB[3]); PIN(accB[3]); SBAR();
            wf1 = trfrag(r2[1] + on, r2[1] + on + 1024);
            if constexpr (j == 6) {
                asm volatile("s_waitcnt vmcnt(12)" : "+v"(fl) :: "memory");
                if (it == 1) {
                    if (!__all(fl == tag)) wait_flags<4>(p.ws + f2p, tag);
                    hso = (80 + 8 * w) * 1024; } }
            hld2(hf[j][0], hf[j][1], lv, ha, hb, hso); SBAR();
        });
    }
    FSTAMP(8);
    const unsigned f3p = F3_OFF + ((tt0 >> 1) * 32 + w * 8 + (l & 7)) * FLS;
    u64 f3 = poll_issue(rs, f3p);
    u32x4 wfo[8][2];
    wfo[0][0] = wf0; wfo[0][1] = wf1;
    unsigned r3[2] = {r2[0] + 40960, r2[1] + 40960};
    PIN(r3[0]); PIN(r3[1]);
    auto rdo = [&](auto M) { constexpr int j = M.value, on = (j < 5 ? j : j - 3) * 8192;
        const unsigned a0 = j < 5 ? r2[0] : r3[0], a1 = j < 5 ? r2[1] : r3[1];
        wfo[j][0] = trfrag(a0 + on, a0 + on + 1024); wfo[j][1] = trfrag(a1 + on, a1 + on + 1024); };
    sfor<5, 8>(rdo);
    { const float gt = 1.0f - get_gate(rs, f2p, fl, tag32(epoch));
      const auto g2 = __builtin_amdgcn_permlane32_swap(__float_as_uint(gt), __float_as_uint(gt), false, false);
      for (int i = 0; i < 4; ++i) { accB[i] *= __uint_as_float(g2[i & 1]); PIN(accB[i]); } }
    const rsrc_t oa = mkrs(p.ws + OF_OFF + ((size_t)(tt0 + wt) * 32 + w * 8) * 1024, 8 * 1024), ob = mkrs(p.ws + OF_OFF + ((size_t)(tt0 + 1 - wt) * 32 + w * 8) * 1024, 8 * 1024);
    auto o_loads = [&]() {
        __builtin_amdgcn_s_setprio(3);
        unsigned oso = 0;
        sfor<0, 4>([&](auto J) { hld2o(hf[J.value][0], hf[J.value][1], lv, ob, oso); });
        oso = 0;
        sfor<4, 8>([&](auto J) { hld2o(hf[J.value][0], hf[J.value][1], lv, oa, oso); }); };
    sfor<0, 7>([&](auto J) { waitv2<0>(hf[J.value][0], hf[J.value][1]); });
    PIN(f3);
    const bool late = __all(f3 == tag);
    if (late) o_loads();
    sfor<1, 5>(rdo);
    const rsrc_t ors = mkrs(p.out, 2048 * 1024);
    unsigned orow = ((tt0 + wt) * 32 + (l & 31)) * 1024 + (n0 + wk * 32) * 2 + h * 16;
    unsigned xw = wdst + lv, xr[3] = {ring + (w ^ 1) * 2048 + lv, ring + (w ^ 2) * 2048 + 16384 + lv, ring + (w ^ 3) * 2048 + 32768 + lv};
    PIN(orow); PIN(xw); PIN(xr[0]); PIN(xr[1]); PIN(xr[2]);
    for (int m = 0; m < 8; ++m) { PIN(wfo[m][0]); PIN(wfo[m][1]); }
    FSTAMP(11);
    if (!late) { wait_flags<1>(p.ws + f3p, tag); o_loads(); }
    FSTAMP(9);
    auto put = [&](auto I, auto R) { constexpr int i = I.value, r = R.value;
        f32x4 v = {accB[i][4 * r], accB[i][4 * r + 1], accB[i][4 * r + 2], accB[i][4 * r + 3]};
        ldsw128(xw + (i - 1) * 16384 + (r >> 1) * 8192 + (r & 1) * 1024, __builtin_bit_cast(u32x4, v)); };
    f32x4 pv[3][4];
    auto get = [&](auto I) { constexpr int i = I.value;
        for (int r = 0; r < 4; ++r) pv[i - 1][r] = __builtin_bit_cast(f32x4, ldsr128(xr[i - 1] + (r >> 1) * 8192 + (r & 1) * 1024)); };
    sfor<0, 8>([&](auto MM) { constexpr int m = MM.value;
        if constexpr ((m & 1) == 0) waitv2<14 - m>(hf[m >> 1][0], hf[m >> 1][1]);
        accB[3] = MFMA(BF8(wfo[m][1]), BF8(hf[m >> 1][m & 1]), accB[3]);
        accB[1] = MFMA(BF8(wfo[m][0]), BF8(hf[m >> 1][m & 1]), accB[1]);
        SBAR(); });
    sfor<0, 8>([&](auto MM) { constexpr int m = MM.value;
        if constexpr ((m & 1) == 0) waitv2<6 - m>(hf[4 + (m >> 1)][0], hf[4 + (m >> 1)][1]);
        accB[2] = MFMA(BF8(wfo[m][1]), BF8(hf[4 + (m >> 1)][m & 1]), accB[2]); SBAR();
        if constexpr (m >= 1 && m < 3) { put(IC<3>{}, IC<2 * m - 2>{}); put(IC<3>{}, IC<2 * m - 1>{}); }
        if constexpr (m >= 3 && m < 5) { put(IC<1>{}, IC<2 * m - 6>{}); put(IC<1>{}, IC<2 * m - 5>{}); }
        if constexpr (m == 6) { lds_bar(); get(IC<3>{}); get(IC<1>{}); }
        SBAR(); });
    sfor<0, 8>([&](auto MM) { constexpr int m = MM.value;
        accB[0] = MFMA(BF8(wfo[m][0]), BF8(hf[4 + (m >> 1)][m & 1]), accB[0]); SBAR();
        if constexpr (m == 1) sfor<0, 4>([&](auto R) { put(IC<2>{}, R); });
        if constexpr (m == 2 || m == 4) for (int r = m - 2; r < m; ++r) { pv[0][r] += pv[2][r]; PIN(pv[0][r]); }
        if constexpr (m == 3) { lds_bar(); get(IC<2>{}); }
        if constexpr (m == 6 || m == 7) for (int r = 2 * m - 12; r < 2 * m - 10; ++r) { pv[0][r] += pv[1][r]; PIN(pv[0][r]); }
        SBAR(); });
    for (int i = 0; i < 4; i += 2) {
        const f32x4 c = pv[0][i] + f32x4{accB[0][4 * i], accB[0][4 * i + 1], accB[0][4 * i + 2], accB[0][4 * i + 3]};
        const f32x4 d = pv[0][i + 1] + f32x4{accB[0][4 * i + 4], accB[0][4 * i + 5], accB[0][4 * i + 6], accB[0][4 * i + 7]};
        auto r0 = __builtin_amdgcn_permlane32_swap(cvtpk(c[0], c[1]), cvtpk(d[0], d[1]), false, false);
        auto r1 = __builtin_amdgcn_permlane32_swap(cvtpk(c[2], c[3]), cvtpk(d[2], d[3]), false, false);
        u32x4 v = {r0[0], r1[0], r0[1], r1[1]};
        __builtin_amdgcn_raw_buffer_store_b128(v, ors, orow + i * 16, 0, 0);
        asm volatile("s_nop 1" :: "v"(v) : "memory");
    }
    FSTAMP(10);
}

DI f32x16 smf(u32x4 a, f32x8 b, f32x16 c, int idx) {
    return __builtin_amdgcn_smfmac_f32_32x32x32_bf16(BF8(a), __builtin_bit_cast(bf16x16, b), c, idx, 0, 0);
}
DI f32x8 lo8(const f32x16& s) { return __builtin_shufflevector(s, s, 0, 1, 2, 3, 4, 5, 6, 7); }
DI f32x8 hi8(const f32x16& s) { return __builtin_shufflevector(s, s, 8, 9, 10, 11, 12, 13, 14, 15); }
template <int B, int E> DI void exps(f32x16& s) {
#pragma unroll
    for (int i = B; i < E; ++i) { float t = __builtin_amdgcn_exp2f(s[i]); PIN(t); s[i] = t; }
}
template <int OFF> DI void spf(unsigned& d, rsrc_t rs, unsigned so) {
    asm volatile("s_buffer_load_dword %0, %1, %2 offset:%3" : "+s"(d) : "s"(rs), "s"(so), "n"(OFF) : "memory");
}
DI float rowmax16(const f32x16& s) {
    float a = __builtin_fmaxf(s[0], s[1]);
#pragma unroll
    for (int i = 2; i < 16; ++i) a = __builtin_fmaxf(a, s[i]);
    return a;
}

DI void a_role(const Params& p, int ai, char* lds, unsigned cpf) {
    const int tid = threadIdx.x, l = tid & 63, w = __builtin_amdgcn_readfirstlane(tid >> 6);
    const int head = (ai & 7) * 4 + ((ai >> 3) & 3), qb = ai >> 5;
    const unsigned base = (unsigned)(uintptr_t)lds;
    const rsrc_t rs = mkrs(p.ws, WS_USED);
    const rsrc_t rkv = mkrs(p.ws + KV_OFF + (size_t)(head * 64 + w * 16) * KVT, 16 * KVT);
    const unsigned lv = l * 16;
    FSTAMP(0);
    auto wpf = [&]() {
      const rsrc_t r2 = mkrs(w == 0 ? p.wg : p.wu, w < 2 ? 1792 * 1024 : 0);
      const int ln = l & 31;
      cpf = __builtin_amdgcn_raw_buffer_load_b32(r2, sel(ln < 16 || (ln >= 20 && ln < 28), (l >> 5) * 3584 + ln * 128, OOR), ai * 7168, 0); MBAR(); };
    const u64 epoch = rfl64(ld_flag(p.ws + FL_OFF));
    const u64 tag = mix64(epoch);
    {
      const int tok = ai * 8 + w * 2 + (l >> 5);
      const u32x4* xr = (const u32x4*)(p.x + (size_t)tok * 1024 + (l & 31) * 32); const u32x4* gr = (const u32x4*)(p.gw + (l & 31) * 32);
      const u32x4 x0 = xr[0], x1 = xr[1], g0 = gr[0], g1 = gr[1];
      float z = 0.f;
      for (int i = 0; i < 4; ++i) {
          z += __uint_as_float(x0[i] << 16) * __uint_as_float(g0[i] << 16) + __uint_as_float(x0[i] & 0xffff0000u) * __uint_as_float(g0[i] & 0xffff0000u);
          z += __uint_as_float(x1[i] << 16) * __uint_as_float(g1[i] << 16) + __uint_as_float(x1[i] & 0xffff0000u) * __uint_as_float(g1[i] & 0xffff0000u); }
      for (int d = 1; d < 32; d <<= 1) z += __shfl_xor(z, d);
      st_flag(rs, sel((l & 31) == 0, GT_OFF + tok * 8, OOR), ((u64)tag32(epoch) << 32) | __float_as_uint(1.0f / (1.0f + __expf(-z)))); }
    PIN(cpf);
    lds_bar();
    st_flag(rs, sel(tid == 0, F0_OFF + (256 + ai) * 8, OOR), tag);
    FSTAMP(5);
    auto bump = [&](unsigned lv) {
        if (ai == 0 && w == 0) {
            const u32x2 t2 = __builtin_bit_cast(u32x2, tag);
#pragma nounroll
            for (unsigned spins = 0; spins < SPIN_CAP; ++spins) {
                unsigned d = 0;
                for (int i = 0; i < 4; ++i) { const u32x4 f = ld16_dev(rs, lv, F0_OFF + i * 1024); d |= (f[0] ^ t2[0]) | (f[1] ^ t2[1]) | (f[2] ^ t2[0]) | (f[3] ^ t2[1]); }
                if (__all(d == 0)) break;
                asm volatile("s_sleep 8" ::: "memory");
            }
            st_flag(rs, sel(lv == 0, FL_OFF, OOR), epoch + 1);
        } };
    const int g = (l >> 4) & 1;
    const unsigned oo = ((l & 11) == 0) ? 2048u : OOR;
    const unsigned v0o = g ? oo : 1024u + ((l >> 5) * 16 + (l & 15)) * 16, v1o = g ? oo : v0o + 512;
    int idx = 0xDDDD; PIN(idx);
    f32x16 o[8] = {};
    for (int i = 0; i < 8; ++i) PIN(o[i]);
    wpf(); PIN(cpf);
    __builtin_amdgcn_s_setprio(APRIO);
    FSTAMP(6);
    unsigned fv = sel(l < 10, F1_OFF + ((head >> 3) * 16 + sel(l & 8, qb * 2 + (l & 1), 64 + (l & 4) * 16 + w * 4 + (l & 3))) * FLS, OOR);
    auto ld_f1 = [&]() { return __builtin_bit_cast(u64, __builtin_amdgcn_raw_buffer_load_b64(rs, fv, 0, 16)); };
    auto kv_ready = [&](u64 f) { const unsigned m = (unsigned)__ballot(f == tag); return m & (m >> 4) & 15u; };
    const unsigned b0 = qb & 3;
    unsigned so = b0 * 4 * KVT, pn = 2 * b0 + 1;
    unsigned go = 1;
    const bool iswo = ai >= 192;
    unsigned sdum = 0, po = iswo ? (ai * 4 + w - 768) * 2048 : min((ai * 4 + w) * 2560, 1792 * 1024 - 2560), pend = po + (iswo ? 2048 : 2560) - SPFN * SPFG;
    rsrc_t pd = mkrs(iswo ? p.wo : p.wd, iswo ? 512 * 1024 : 1792 * 1024);
    const unsigned qo = QF_OFF + (head * 64 + qb * 8 + (6 ^ (2 * w))) * 1024;
    PINS(so); PINS(pn); PINS(go); PINS(sdum); PINS(po); PINS(pend); PINS(pd);
    unsigned ak = lv, a0 = v0o, a1 = v1o;
    PIN(ak); PIN(a0); PIN(a1);
    const unsigned gv = GT_OFF + ((qb * 4 + w) * 64 + l) * 8;
    float gt = get_gate(rs, gv, __builtin_bit_cast(u64, __builtin_amdgcn_raw_buffer_load_b64(rs, gv, 0, 16)), tag32(epoch)); PIN(gt);
    wait_flags2<W1A, W1B>(rs, sel((l & 8) || (l & 3) == b0, fv, OOR), tag);
    FSTAMP(1);
    u32x4 qf[8], K[2], V0[2], V1[2];
    K[0] = ld16_dev(rkv, ak, so);
    for (int i = 0; i < 2; ++i) qf[i] = ld16_dev(rs, ak, qo ^ (i * 1024));
    MBAR();
    V0[0] = ld16_dev(rkv, a0, so); MBAR();
    V1[0] = ld16_dev(rkv, a1, so); MBAR();
    for (int i = 2; i < 8; ++i) qf[i] = ld16_dev(rs, ak, qo ^ (i * 1024));
    MBAR();
    K[1] = ld16_dev(rkv, ak + KVT, so); MBAR();
    V0[1] = ld16_dev(rkv, a0 + KVT, so); MBAR();
    fv = sel(l < 8, fv, OOR); PIN(fv);
    u64 fl = 0;
    auto put = [&](auto I) { constexpr int i = I.value, j = i >> 1;
        const unsigned pa = base + ((j ^ 3 ^ w) * 3 + j) * 4096 + ak, la = base + 65536 + ((j ^ 3 ^ w) * 3 + j) * 512 + (ak >> 1);
        f32x4 x0 = {o[i][0], o[i][1], o[i][2], o[i][3]}, x1 = {o[i][4], o[i][5], o[i][6], o[i][7]};
        ldsw128(pa + (i & 1) * 2048, __builtin_bit_cast(u32x4, x0)); ldsw128(pa + (i & 1) * 2048 + 1024, __builtin_bit_cast(u32x4, x1));
        if constexpr (i & 1) { const u32x2 s2 = {__float_as_uint(o[i - 1][8]), __float_as_uint(o[i][8])};
            *(LAS u32x2*)(uintptr_t)la = s2; } };
    u32x2 s2[3]; u32x4 xa[2][6];
    auto gather = [&]() {
        unsigned gl = base + 65536 + w * 1536 + (ak >> 1), ga = base + w * 12288 + ak;
        PIN(gl); PIN(ga);
        lds_bar();
        for (int v = 0; v < 3; ++v) s2[v] = *(const LAS u32x2*)(uintptr_t)(gl + v * 512);
        for (int u = 0; u < 2; ++u) for (int v = 0; v < 6; ++v) xa[u][v] = ldsr128(ga + u * 2048 + (v >> 1) * 4096 + (v & 1) * 1024);
        MBAR(); SBAR(); };
    { f32x16 sc = MFMA(BF8(K[0]), BF8(qf[0]), f32x16{}), sn;
      SBAR();
      f32x8 ph = {}; V1[1] = u32x4{}; PIN(ph); PIN(V1[1]);
      for (int i = 2; i < 8; ++i) PIN(qf[i]);
      FSTAMP(2);
      auto tile = [&](auto T, auto FIN) {
          constexpr int t = T.value, tn = t ^ 1;
          constexpr bool more = !FIN.value, last = FIN.value && t == 1;
          sfor<0, 8>([&](auto I) {
              constexpr int i = I.value, in = (i + 1) & 7, ip = (i + 7) & 7;
              if constexpr (i == 7 && !last) PIN(V1[tn]);
              SBAR();
              exps<0, P0>(sc); SBAR();
              if constexpr (i < 7 || !last) { sn = MFMA(BF8(K[i < 7 ? t : tn]), BF8(qf[in]), f32x16{}); SBAR(); }
              if constexpr ((t * 8 + i) % (16 / SPFN) == 0 && more) { spf<(t * 8 + i) / (16 / SPFN) * SPFG>(sdum, pd, po); SBAR(); }
              if constexpr (i == 6 && more) { K[t] = ld16_dev(rkv, ak + t * KVT, so); SBAR(); }
              if constexpr (last && i >= 2 && i < 7) { put(IC<i - 2>{}); SBAR(); }
              if constexpr (last && i == 7) { gather(); PINS(sdum); }
              exps<P0, P1>(sc); SBAR();
              o[ip] = smf(V1[i == 0 ? tn : t], ph, o[ip], idx); SBAR();
              if constexpr (i == 0 && !last) { V1[tn] = ld16_dev(rkv, a1 + tn * KVT, so); SBAR(); }
              if constexpr (t == 0 && i == 1 && more) { so = pn * 2 * KVT; PINS(so); SBAR(); }
              if constexpr (t == 0 && i == 2 && more) { pn = (pn + 1) & 7; go >>= 1; PINS(pn); PINS(go); SBAR(); }
              if constexpr (t == 1 && i == 7 && more) { po = po + SPFN * SPFG < pend ? po + SPFN * SPFG : pend; PINS(po); SBAR(); }
              if constexpr (t == 1 && i == 1 && more) { fl = ld_f1(); SBAR(); }
              exps<P1, 8>(sc); SBAR();
              exps<8, P2>(sc); SBAR();
              o[i] = smf(V0[t], lo8(sc), o[i], idx); SBAR();
              if constexpr (i == 7 && more) { V0[t] = ld16_dev(rkv, a0 + t * KVT, so); SBAR(); }
              if constexpr (last && i == 6) { exps<P2, 13>(sc); SBAR(); put(IC<5>{}); SBAR(); exps<13, 16>(sc); SBAR(); }
              else { exps<P2, 16>(sc); SBAR(); }
              ph = hi8(sc); sc = sn;
          }); };
      for (;;) {
          PIN(ak); PIN(a0); PIN(a1);
          tile(IC<0>{}, IC<0>{});
          tile(IC<1>{}, IC<0>{});
          if (__builtin_expect(!(go & 1), 0)) {
              if (pn == 2 * b0) break;
              unsigned rdy = kv_ready(fl);
#pragma nounroll
              for (unsigned spins = 0; spins < SPIN_CAP && !((rdy >> (pn >> 1)) & 1); ++spins) {
                  asm volatile("s_sleep 2" ::: "memory");
                  rdy = kv_ready(ld_f1());
              }
              if (rdy == 15) fv = OOR;
              const unsigned k = (pn - 2 * b0 - 1) & 7, e = ((rdy & 1) | (rdy & 2) << 1 | (rdy & 4) << 2 | (rdy & 8) << 3) * 0x303;
              go = (e >> pn) & (0x7fu >> k);
          }
      }
      tile(IC<0>{}, IC<1>{});
      tile(IC<1>{}, IC<1>{});
      o[7] = smf(V1[1], ph, o[7], idx); SBAR(); }
    FSTAMP(3);
    float ls[2], big = 0.f;
    auto finish = [&]() {
        const auto g2 = __builtin_amdgcn_permlane32_swap(__float_as_uint(gt), __float_as_uint(gt), false, false);
        sfor<0, 2>([&](auto U) { constexpr int u = U.value;
            auto F = [&](int v) { return __builtin_bit_cast(f32x4, xa[u][v]); };
            ls[u] = (__uint_as_float(s2[0][u]) + __uint_as_float(s2[1][u])) + (__uint_as_float(s2[2][u]) + o[6 + u][8]);
            const float r = __builtin_amdgcn_rcpf(ls[u]) * __uint_as_float(g2[u]);
            const f32x4 x0 = (F(0) + F(2)) + (F(4) + f32x4{o[6 + u][0], o[6 + u][1], o[6 + u][2], o[6 + u][3]});
            const f32x4 x1 = (F(1) + F(3)) + (F(5) + f32x4{o[6 + u][4], o[6 + u][5], o[6 + u][6], o[6 + u][7]});
            u32x4 v = {cvtpk(x0[0] * r, x0[1] * r), cvtpk(x0[2] * r, x0[3] * r), cvtpk(x1[0] * r, x1[1] * r), cvtpk(x1[2] * r, x1[3] * r)};
            st16_wt(rs, ak, OF_OFF + ((qb * 8 + w * 2 + u) * 32 + head) * 1024, v);
            asm volatile("" :: "v"(o[6 + u]));
            xa[u][0] = __builtin_bit_cast(u32x4, x0); xa[u][1] = __builtin_bit_cast(u32x4, x1); SBAR(); }); };
    finish();
    {   for (int u = 0; u < 2; ++u) { big += ls[u] * 1.0e19f;
            for (int r = 0; r < 4; ++r) big += __builtin_fabsf(__uint_as_float(xa[u][0][r])) + __builtin_fabsf(__uint_as_float(xa[u][1][r])); }
        *(LAS int*)(uintptr_t)(base + 73728 + w * 4) = __any(!(big < 1.0e37f) || !(__builtin_fminf(ls[0], ls[1]) > 1.0e-9f)) ? 1 : 0;
        lds_bar();
    }
    const u32x4 bw = ldsr128(base + 73728);
    if (__builtin_expect(__builtin_amdgcn_readfirstlane((int)(bw[0] | bw[1] | bw[2] | bw[3])), 0)) {
        float mx[8];
        for (int i = 0; i < 8; ++i) mx[i] = -__builtin_inff();
#pragma nounroll
        for (int t = 0; t < 16; ++t) {
            const u32x4 kf = ld16_dev(rkv, ak, t * KVT);
            for (int i = 0; i < 8; ++i) mx[i] = __builtin_fmaxf(mx[i], rowmax16(MFMA(BF8(kf), BF8(qf[i]), f32x16{})));
        }
        for (int i = 0; i < 8; ++i) *(LAS float*)(uintptr_t)(base + (w * 8 + (i ^ (2 * w))) * 256 + (ak >> 2)) = mx[i];
        lds_bar();
        for (int i = 0; i < 8; ++i) {
            for (int v = 0; v < 8; ++v) mx[i] = __builtin_fmaxf(mx[i], *(const LAS float*)(uintptr_t)(base + ((v >> 1) * 8 + (i ^ (2 * w))) * 256 + (v & 1) * 128 + ((ak >> 2) & 127)));
            o[i] = f32x16{}; }
        lds_bar();
#pragma nounroll
        for (int t = 0; t < 16; ++t) {
            const u32x4 kf = ld16_dev(rkv, ak, t * KVT), v0 = ld16_dev(rkv, a0, t * KVT), v1 = ld16_dev(rkv, a1, t * KVT);
            for (int i = 0; i < 8; ++i) {
                f32x16 s = MFMA(BF8(kf), BF8(qf[i]), f32x16{});
                for (int r = 0; r < 16; ++r) s[r] = __builtin_amdgcn_exp2f(s[r] - mx[i]);
                o[i] = smf(v0, lo8(s), o[i], idx); o[i] = smf(v1, hi8(s), o[i], idx); SBAR(); }
        }
        sfor<0, 6>(put);
        gather();
        finish();
    }
    waitv<0>();
    st_flag(rs, sel(ak == 0, F3_OFF + ((qb * 4 + w) * 32 + head) * FLS, OOR), tag);
    FSTAMP(4);
    bump(ak);
}

__global__ __launch_bounds__(256, 2) void k_fused(Params p) {
    extern __shared__ __attribute__((aligned(16))) char lds[];
    if (blockIdx.x < 256) { g_role(p, blockIdx.x, lds); asm volatile(".Lkf_end_g:"); __builtin_amdgcn_endpgm(); }
    unsigned cpf = 0;
    {
      u64 pc; int len;
      asm volatile("s_getpc_b64 %0\n.Lkf_pc:\n\ts_mov_b32 %1, .Lkf_end_g-.Lkf_pc" : "=s"(pc), "=s"(len));
      const rsrc_t cr = mkrs((const char*)pc, len > 0 ? len : 0);
      unsigned t = __builtin_amdgcn_raw_buffer_load_b32(cr, sel(threadIdx.x < 64, (((blockIdx.x >> 3) & 7) * 64 + threadIdx.x) * 128, OOR), 0, 0);
      cpf = t; MBAR(); }
    a_role(p, blockIdx.x - 256, lds, cpf);
}

extern "C" void kernel_launch(void* const* d_in, const int* in_sizes, int n_in, void* d_out, int out_size, void* d_ws, size_t ws_size,
                              hipStream_t stream) {
    Params p;
    p.x = (const char*)d_in[0]; p.wqkv = (const char*)d_in[1]; p.wo = (const char*)d_in[2];
    p.wg = (const char*)d_in[4]; p.wu = (const char*)d_in[5]; p.wd = (const char*)d_in[6]; p.gw = (const char*)d_in[7];
    p.out = (char*)d_out; p.ws = (char*)d_ws;
    (void)hipFuncSetAttribute((const void*)k_fused, hipFuncAttributeMaxDynamicSharedMemorySize, LDS_BYTES);
    k_fused<<<512, 256, LDS_BYTES, stream>>>(p);
}
```
